```python
import torch

def _softmax(x, axis=-1):
    return torch.softmax(x, dim=axis)
def _gelu(x):
    return torch.nn.functional.gelu(x, approximate="tanh")
def _silu(x):
    return torch.nn.functional.silu(x)
def _sigmoid(x):
    return torch.sigmoid(x)
def _layernorm(x, axis=-1, eps=1e-5):
    m = x.mean(dim=axis, keepdim=True)
    v = x.var(dim=axis, keepdim=True, unbiased=False)
    return (x - m) / torch.sqrt(v + eps)
def _scatter_add(tgt, idx, src, axis=0):
    return tgt.clone().index_add_(axis, idx, src)
def _segment_sum(src, seg, n):
    out = torch.zeros((n,) + src.shape[1:], dtype=src.dtype)
    return out.index_add_(0, seg, src)
def _pad(x, pw):
    flat = tuple(v for pair in reversed(pw) for v in pair)
    return torch.nn.functional.pad(x, flat)

kernel_name = 'standalone__gqa_softmax__near_ridge'

def setup_inputs():

    _w = (lambda a: (lambda t: t.to(torch.bfloat16) if t.is_floating_point() else t)(torch.as_tensor(a)))
    B, Hq, Hkv, Sq, Sk, D = 16, 16, 8, 384, 384, 256
    return {
        "q": _w((lambda *s: torch.randn(*s, dtype=torch.bfloat16) * 0.8)(B, Hq, Sq, D)),
        "k": _w((lambda *s: torch.randn(*s, dtype=torch.bfloat16) * 0.8)(B, Hkv, Sk, D)),
        "v": _w((lambda *s: torch.randn(*s, dtype=torch.bfloat16) * 0.05)(B, Hkv, Sk, D)),
    }

def reference(q, k, v):
    B, Hq, Sq, D = q.shape
    Hkv = k.shape[1]
    rep = Hq // Hkv
    k = (lambda x, n, axis: torch.repeat_interleave(x, n, dim=axis))(k, rep, axis=1)
    v = (lambda x, n, axis: torch.repeat_interleave(x, n, dim=axis))(v, rep, axis=1)
    scale = D ** -0.5
    scores = torch.einsum("bhqd,bhkd->bhqk", q, k) * scale
    p = _softmax(scores, axis=-1)
    return torch.einsum("bhqk,bhkd->bhqd", p, v)
```

```cpp
#include <hip/hip_runtime.h>
#include <hip/hip_bf16.h>
using bf16x8 = __attribute__((ext_vector_type(8))) short;
using f32x16 = __attribute__((ext_vector_type(16))) float;
using u32x4 = __attribute__((ext_vector_type(4))) unsigned;
constexpr int B=16,HQ=16,HKV=8,SQ=384,SK=384,D=256;
__device__ __forceinline__ unsigned cvtpk(float lo, float hi){ unsigned r; asm volatile("v_cvt_pk_bf16_f32 %0, %1, %2":"=v"(r):"v"(lo),"v"(hi)); return r;}
__global__ __launch_bounds__(64,1) void k0(const short* __restrict__ Q,const short* __restrict__ K,const short* __restrict__ V, unsigned short* __restrict__ O){
  int lane=threadIdx.x, r=lane&31,h=lane>>5;
  int blk=blockIdx.x; int qt=blk%12; int bh=blk/12; int b=bh/HQ, hq=bh%HQ, hk=hq/2;
  const short* q=Q+((long)bh*SQ+qt*32+r)*D; const short* k=K+((long)(b*HKV+hk)*SK)*D; const short* v=V+((long)(b*HKV+hk)*SK)*D;
  f32x16 s[12];
  #pragma unroll
  for(int i=0;i<12;i++){ for(int j=0;j<16;j++) s[i][j]=0.f; }
  #pragma unroll 1
  for(int ks=0;ks<16;ks++){
    bf16x8 qf=*(const bf16x8*)(q+ks*16+8*h);
    #pragma unroll
    for(int i=0;i<12;i++){ bf16x8 kf=*(const bf16x8*)(k+(long)(i*32+r)*D+ks*16+8*h); s[i]=__builtin_amdgcn_mfma_f32_32x32x16_bf16(kf,qf,s[i],0,0,0);}
  }
  float m=-1e30f;
  #pragma unroll
  for(int i=0;i<12;i++) for(int j=0;j<16;j++) m=fmaxf(m,s[i][j]);
  m=fmaxf(m,__shfl_xor(m,32));
  const float c=0.0625f*1.4426950408889634f; float l=0;
  #pragma unroll
  for(int i=0;i<12;i++) for(int j=0;j<16;j++){ s[i][j]=__builtin_amdgcn_exp2f((s[i][j]-m)*c); l+=s[i][j];}
  l+=__shfl_xor(l,32);
  float rl=1.f/l;
  bf16x8 p[24];
  #pragma unroll
  for(int i=0;i<12;i++) for(int t=0;t<2;t++){ u32x4 w; for(int e=0;e<4;e++) w[e]=cvtpk(s[i][8*t+2*e],s[i][8*t+2*e+1]); asm volatile("s_nop 1"); p[2*i+t]=__builtin_bit_cast(bf16x8,w);}
  #pragma unroll 1
  for(int db=0;db<8;db++){
    f32x16 o; for(int j=0;j<16;j++) o[j]=0.f;
    #pragma unroll
    for(int t=0;t<24;t++){ bf16x8 vf; for(int j=0;j<8;j++){ int key=16*t+8*(j>>2)+4*h+(j&3); vf[j]=v[(long)key*D+db*32+r]; }
      o=__builtin_amdgcn_mfma_f32_32x32x16_bf16(vf,p[t],o,0,0,0);}
    unsigned short* op=O+((long)bh*SQ+qt*32+r)*D+db*32;
    for(int g=0;g<4;g++){ unsigned a=cvtpk(o[4*g]*rl,o[4*g+1]*rl), bb=cvtpk(o[4*g+2]*rl,o[4*g+3]*rl); *(uint2*)(op+8*g+4*h)=make_uint2(a,bb);}
  }
}
extern "C" void kernel_launch(void* const* d_in,const int* in_sizes,int n_in,void* d_out,int out_size,void* d_ws,size_t ws_size,hipStream_t stream){
  k0<<<B*HQ*12,64,0,stream>>>((const short*)d_in[0],(const short*)d_in[1],(const short*)d_in[2],(unsigned short*)d_out);
}
```

```cpp
#include <hip/hip_runtime.h>
#include <hip/hip_bf16.h>

extern "C" __global__ __launch_bounds__(256, 1) void gqa_kvs_kernel(const void* __restrict__ q, const void* __restrict__ k,
                                                                   const void* __restrict__ v, void* __restrict__ o) {
  __shared__ __attribute__((aligned(16))) char lds[163840];
  asm volatile("" ::"v"(lds) : "memory");
  const unsigned long long qa = (unsigned long long)q, ka = (unsigned long long)k, va = (unsigned long long)v,
                           oa = (unsigned long long)o;
  asm volatile(
      "v_lshrrev_b32 v1, 6, %0\n"
      "s_and_b32 s63, %9, 7\n"
      "s_lshr_b32 s64, %9, 3\n"
      "v_readfirstlane_b32 s52, v1\n"
      "v_mov_b32 v14, %0\n"
      "v_and_b32 v0, 63, %0\n"
      "s_lshl_b32 s63, s63, 4\n"
      "s_lshr_b32 s65, s64, 1\n"
      "s_add_u32 s63, s63, s65\n"
      "s_and_b32 s64, s64, 1\n"
      "s_lshl_b32 s65, s63, 1\n"
      "s_add_u32 s64, s64, s65\n"
      "s_mul_i32 s63, s63, 196608\n"
      "s_mul_i32 s64, s64, 196608\n"
      "s_add_u32 s36, %1, s64\n"
      "s_addc_u32 s37, %2, 0\n"
      "s_and_b32 s37, s37, 0xffff\n"
      "s_mov_b32 s38, 196608\n"
      "s_mov_b32 s39, 0x20000\n"
      "s_add_u32 s40, %3, s63\n"
      "s_addc_u32 s41, %4, 0\n"
      "s_and_b32 s41, s41, 0xffff\n"
      "s_mov_b32 s42, 196608\n"
      "s_mov_b32 s43, 0x20000\n"
      "s_add_u32 s44, %5, s63\n"
      "s_addc_u32 s45, %6, 0\n"
      "s_and_b32 s45, s45, 0xffff\n"
      "s_mov_b32 s46, 196608\n"
      "s_mov_b32 s47, 0x20000\n"
      "s_add_u32 s48, %7, s64\n"
      "s_addc_u32 s49, %8, 0\n"
      "s_and_b32 s49, s49, 0xffff\n"
      "s_mov_b32 s50, 196608\n"
      "s_mov_b32 s51, 0x20000\n"
      "v_lshrrev_b32 v1, 5, v0\n"
      "v_and_b32 v2, 31, v0\n"
      "v_lshlrev_b32 v1, 12, v1\n"
      "v_lshl_add_u32 v102, v2, 4, v1\n"
      "s_mul_i32 s53, s52, 27456\n"
      "s_add_u32 s53, s53, 33792\n"
      "s_mul_i32 s54, s52, 8448\n"
      "s_lshl_b32 s57, s52, 10\n"
      "s_mul_i32 s65, s52, 49152\n"
      "s_add_u32 m0, s53, 16896\n"
      "s_nop 0\n"
      "buffer_load_dwordx4 v102, s[36:39], s57 offen nt lds\n"
      "s_add_u32 m0, m0, 544\n"
      "s_nop 0\n"
      "buffer_load_dwordx4 v102, s[36:39], s57 offen offset:512 nt lds\n"
      "s_add_u32 s57, s57, 8192\n"
      "s_add_u32 m0, s53, 19008\n"
      "s_nop 0\n"
      "buffer_load_dwordx4 v102, s[36:39], s57 offen nt lds\n"
      "s_add_u32 m0, m0, 544\n"
      "s_nop 0\n"
      "buffer_load_dwordx4 v102, s[36:39], s57 offen offset:512 nt lds\n"
      "s_add_u32 s57, s57, 8192\n"
      "s_add_u32 m0, s54, 0\n"
      "s_add_u32 s64, s65, 0\n"
      "buffer_load_dwordx4 v102, s[40:43], s64 offen nt lds\n"
      "s_add_u32 m0, m0, 544\n"
      "s_nop 0\n"
      "buffer_load_dwordx4 v102, s[40:43], s64 offen offset:512 nt lds\n"
      "s_add_u32 m0, m0, 544\n"
      "s_nop 0\n"
      "buffer_load_dwordx4 v102, s[40:43], s64 offen offset:1024 nt lds\n"
      "s_add_u32 m0, m0, 544\n"
      "s_nop 0\n"
      "buffer_load_dwordx4 v102, s[40:43], s64 offen offset:1536 nt lds\n"
      "s_add_u32 m0, m0, 544\n"
      "s_nop 0\n"
      "buffer_load_dwordx4 v102, s[40:43], s64 offen offset:2048 nt lds\n"
      "s_add_u32 m0, m0, 544\n"
      "s_nop 0\n"
      "buffer_load_dwordx4 v102, s[40:43], s64 offen offset:2560 nt lds\n"
      "s_add_u32 m0, m0, 544\n"
      "s_nop 0\n"
      "buffer_load_dwordx4 v102, s[40:43], s64 offen offset:3072 nt lds\n"
      "s_add_u32 m0, m0, 544\n"
      "s_nop 0\n"
      "buffer_load_dwordx4 v102, s[40:43], s64 offen offset:3584 nt lds\n"
      "s_getpc_b64 s[66:67]\n"
      "6:\n"
      "s_lshr_b32 s63, %9, 3\n"
      "s_lshl_b32 s63, s63, 2\n"
      "s_add_u32 s63, s63, s52\n"
      "s_lshl_b32 s63, s63, 8\n"
      "v_and_b32 v20, 1, v0\n"
      "v_lshlrev_b32 v20, 7, v20\n"
      "v_add_u32 v20, s63, v20\n"
      "v_min_u32 v21, 9f-6b, v20\n"
      "global_load_dword v250, v21, s[66:67]\n"
      "v_and_b32 v3, 15, v0\n"
      "v_lshrrev_b32 v4, 4, v0\n"
      "v_and_b32 v5, 7, v0\n"
      "v_bfe_u32 v6, v0, 3, 1\n"
      "v_lshlrev_b32 v7, 9, v6\n"
      "v_lshl_add_u32 v7, v4, 4, v7\n"
      "v_mul_u32_u24 v8, 1056, v5\n"
      "v_add_u32 v8, v8, v7\n"
      "v_add_u32 v116, s54, v8\n"
      "v_add_u32 v117, s53, v8\n"
      "v_lshrrev_b32 v9, 1, v5\n"
      "v_mul_u32_u24 v9, 27456, v9\n"
      "v_and_b32 v10, 1, v0\n"
      "v_mul_u32_u24 v10, 1056, v10\n"
      "v_add3_u32 v97, v9, v10, v7\n"
      "v_add_u32 v97, 33792, v97\n"
      "v_lshrrev_b32 v9, 1, v5\n"
      "v_mul_u32_u24 v9, 8448, v9\n"
      "v_add3_u32 v127, v9, v10, v7\n"
      "v_add_u32 v127, 6144, v127\n"
      "v_lshlrev_b32 v98, 4, v0\n"
      "v_add_u32 v99, s54, v98\n"
      "v_mul_u32_u24 v9, 144, v3\n"
      "v_lshl_add_u32 v9, v4, 5, v9\n"
      "v_add_u32 v9, 6144, v9\n"
      "v_add_u32 v100, s54, v9\n"
      "v_lshrrev_b32 v11, 3, v0\n"
      "v_mul_u32_u24 v9, 144, v11\n"
      "v_lshl_add_u32 v9, v5, 4, v9\n"
      "v_add_u32 v9, 6144, v9\n"
      "v_add_u32 v101, s54, v9\n"
      "s_lshl_b32 s64, s52, 7\n"
      "v_lshlrev_b32 v9, 9, v11\n"
      "v_add_u32 v9, s64, v9\n"
      "v_lshl_add_u32 v103, v5, 4, v9\n"
      "v_lshlrev_b32 v9, 12, v4\n"
      "v_lshl_add_u32 v9, v6, 9, v9\n"
      "v_add_u32 v9, s64, v9\n"
      "v_lshl_add_u32 v113, v5, 4, v9\n"
      "v_xor_b32 v112, 16, v113\n"
      "v_bfe_u32 v9, v0, 3, 2\n"
      "v_lshlrev_b32 v9, 10, v9\n"
      "v_lshrrev_b32 v10, 5, v0\n"
      "v_lshl_add_u32 v9, v10, 8, v9\n"
      "v_bfe_u32 v10, v0, 2, 1\n"
      "v_lshl_add_u32 v9, v10, 7, v9\n"
      "v_and_b32 v12, 3, v0\n"
      "v_lshlrev_b32 v12, 1, v12\n"
      "v_xor_b32 v12, v12, v6\n"
      "v_lshl_add_u32 v10, v12, 4, v9\n"
      "v_add_u32 v114, s53, v10\n"
      "v_xor_b32 v10, 16, v10\n"
      "v_add_u32 v115, s53, v10\n"
      "v_lshlrev_b32 v9, 2, v3\n"
      "v_add_u32 v104, 143488, v9\n"
      "v_cmp_gt_u32 vcc, 96, v14\n"
      "v_mov_b32 v16, 0xff800000\n"
      "v_lshlrev_b32 v15, 4, v14\n"
      "v_cndmask_b32 v16, 0, v16, vcc\n"
      "v_add_u32 v15, 143616, v15\n"
      "v_mov_b32 v17, v16\n"
      "v_mov_b32 v18, v16\n"
      "v_mov_b32 v19, v16\n"
      "ds_write_b128 v15, v[16:19]\n"
      "v_mov_b32 v111, 0x41a00000\n"
      "s_mov_b32 s55, 0x3db8aa3b\n"
      "s_mov_b32 s56, 0xbdb8aa3b\n"
      "s_mov_b32 s60, 0\n"
      "s_mov_b32 s61, 4096\n"
      "s_mov_b32 s62, 8\n"
      "s_mov_b32 s58, 14784\n"
      "s_mov_b32 s69, 0\n"
      "s_mov_b32 s68, 0\n"
      "v_add_u32 v96, 16896, v97\n"
      "s_add_u32 m0, s53, 0\n"
      "s_add_u32 s64, s65, 8192\n"
      "buffer_load_dwordx4 v102, s[40:43], s64 offen nt lds\n"
      "s_add_u32 m0, m0, 544\n"
      "s_nop 0\n"
      "buffer_load_dwordx4 v102, s[40:43], s64 offen offset:512 nt lds\n"
      "s_add_u32 m0, m0, 544\n"
      "s_nop 0\n"
      "buffer_load_dwordx4 v102, s[40:43], s64 offen offset:1024 nt lds\n"
      "s_add_u32 m0, m0, 544\n"
      "s_nop 0\n"
      "buffer_load_dwordx4 v102, s[40:43], s64 offen offset:1536 nt lds\n"
      "s_add_u32 m0, m0, 544\n"
      "s_nop 0\n"
      "buffer_load_dwordx4 v102, s[40:43], s64 offen offset:2048 nt lds\n"
      "s_add_u32 m0, m0, 544\n"
      "s_nop 0\n"
      "buffer_load_dwordx4 v102, s[40:43], s64 offen offset:2560 nt lds\n"
      "s_add_u32 m0, m0, 544\n"
      "s_nop 0\n"
      "buffer_load_dwordx4 v102, s[40:43], s64 offen offset:3072 nt lds\n"
      "s_add_u32 m0, m0, 544\n"
      "s_nop 0\n"
      "buffer_load_dwordx4 v102, s[40:43], s64 offen offset:3584 nt lds\n"
      "s_waitcnt vmcnt(17) lgkmcnt(0)\n"
      "s_barrier\n"
      "ds_read_b128 v[64:67], v96\n"
      "ds_read_b128 v[68:71], v96 offset:64\n"
      "ds_read_b128 v[72:75], v96 offset:128\n"
      "ds_read_b128 v[76:79], v96 offset:192\n"
      "ds_read_b128 v[80:83], v96 offset:256\n"
      "ds_read_b128 v[84:87], v96 offset:320\n"
      "ds_read_b128 v[88:91], v96 offset:384\n"
      "ds_read_b128 v[92:95], v96 offset:448\n"
      "ds_read_b128 v[128:131], v96 offset:2112\n"
      "ds_read_b128 v[132:135], v96 offset:2176\n"
      "ds_read_b128 v[136:139], v96 offset:2240\n"
      "ds_read_b128 v[140:143], v96 offset:2304\n"
      "ds_read_b128 v[144:147], v96 offset:2368\n"
      "ds_read_b128 v[148:151], v96 offset:2432\n"
      "ds_read_b128 v[152:155], v96 offset:2496\n"
      "ds_read_b128 v[156:159], v96 offset:2560\n"
      "s_waitcnt vmcnt(9)\n"
      "ds_read_b128 a[0:3], v116\n"
      "ds_read_b128 a[4:7], v116 offset:64\n"
      "ds_read_b128 a[8:11], v116 offset:128\n"
      "ds_read_b128 a[12:15], v116 offset:192\n"
      "ds_read_b128 a[16:19], v116 offset:256\n"
      "ds_read_b128 a[20:23], v116 offset:320\n"
      "ds_read_b128 a[24:27], v116 offset:384\n"
      "ds_read_b128 a[28:31], v116 offset:448\n"
      "s_add_u32 m0, s53, 8448\n"
      "s_add_u32 s64, s65, 16384\n"
      "buffer_load_dwordx4 v102, s[40:43], s64 offen nt lds\n"
      "s_add_u32 m0, m0, 544\n"
      "s_nop 0\n"
      "buffer_load_dwordx4 v102, s[40:43], s64 offen offset:512 nt lds\n"
      "s_add_u32 m0, m0, 544\n"
      "s_nop 0\n"
      "buffer_load_dwordx4 v102, s[40:43], s64 offen offset:1024 nt lds\n"
      "s_add_u32 m0, m0, 544\n"
      "s_nop 0\n"
      "buffer_load_dwordx4 v102, s[40:43], s64 offen offset:1536 nt lds\n"
      "s_add_u32 m0, m0, 544\n"
      "s_nop 0\n"
      "buffer_load_dwordx4 v102, s[40:43], s64 offen offset:2048 nt lds\n"
      "s_add_u32 m0, m0, 544\n"
      "s_nop 0\n"
      "buffer_load_dwordx4 v102, s[40:43], s64 offen offset:2560 nt lds\n"
      "s_add_u32 m0, m0, 544\n"
      "s_nop 0\n"
      "buffer_load_dwordx4 v102, s[40:43], s64 offen offset:3072 nt lds\n"
      "s_add_u32 m0, m0, 544\n"
      "s_nop 0\n"
      "buffer_load_dwordx4 v102, s[40:43], s64 offen offset:3584 nt lds\n"
      "s_waitcnt lgkmcnt(7)\n"
      "v_mfma_f32_16x16x32_bf16 v[0:3], a[0:3], v[64:67], 0\n"
      "s_waitcnt lgkmcnt(6)\n"
      "v_mfma_f32_16x16x32_bf16 v[0:3], a[4:7], v[68:71], v[0:3]\n"
      "s_waitcnt lgkmcnt(5)\n"
      "v_mfma_f32_16x16x32_bf16 v[0:3], a[8:11], v[72:75], v[0:3]\n"
      "s_waitcnt lgkmcnt(4)\n"
      "v_mfma_f32_16x16x32_bf16 v[0:3], a[12:15], v[76:79], v[0:3]\n"
      "s_waitcnt lgkmcnt(3)\n"
      "v_mfma_f32_16x16x32_bf16 v[0:3], a[16:19], v[80:83], v[0:3]\n"
      "s_waitcnt lgkmcnt(2)\n"
      "v_mfma_f32_16x16x32_bf16 v[0:3], a[20:23], v[84:87], v[0:3]\n"
      "s_waitcnt lgkmcnt(1)\n"
      "v_mfma_f32_16x16x32_bf16 v[0:3], a[24:27], v[88:91], v[0:3]\n"
      "s_waitcnt lgkmcnt(0)\n"
      "v_mfma_f32_16x16x32_bf16 v[0:3], a[28:31], v[92:95], v[0:3]\n"
      "v_mfma_f32_16x16x32_bf16 v[24:27], a[0:3], v[128:131], 0\n"
      "v_mfma_f32_16x16x32_bf16 v[24:27], a[4:7], v[132:135], v[24:27]\n"
      "v_mfma_f32_16x16x32_bf16 v[24:27], a[8:11], v[136:139], v[24:27]\n"
      "v_mfma_f32_16x16x32_bf16 v[24:27], a[12:15], v[140:143], v[24:27]\n"
      "v_mfma_f32_16x16x32_bf16 v[24:27], a[16:19], v[144:147], v[24:27]\n"
      "v_mfma_f32_16x16x32_bf16 v[24:27], a[20:23], v[148:151], v[24:27]\n"
      "v_mfma_f32_16x16x32_bf16 v[24:27], a[24:27], v[152:155], v[24:27]\n"
      "v_mfma_f32_16x16x32_bf16 v[24:27], a[28:31], v[156:159], v[24:27]\n"
      "s_add_u32 m0, s54, 0\n"
      "s_add_u32 s64, s65, 24576\n"
      "buffer_load_dwordx4 v102, s[40:43], s64 offen nt lds\n"
      "s_add_u32 m0, m0, 544\n"
      "s_nop 0\n"
      "buffer_load_dwordx4 v102, s[40:43], s64 offen offset:512 nt lds\n"
      "s_add_u32 m0, m0, 544\n"
      "s_nop 0\n"
      "buffer_load_dwordx4 v102, s[40:43], s64 offen offset:1024 nt lds\n"
      "s_add_u32 m0, m0, 544\n"
      "s_nop 0\n"
      "buffer_load_dwordx4 v102, s[40:43], s64 offen offset:1536 nt lds\n"
      "s_add_u32 m0, m0, 544\n"
      "s_nop 0\n"
      "buffer_load_dwordx4 v102, s[40:43], s64 offen offset:2048 nt lds\n"
      "s_add_u32 m0, m0, 544\n"
      "s_nop 0\n"
      "buffer_load_dwordx4 v102, s[40:43], s64 offen offset:2560 nt lds\n"
      "s_add_u32 m0, m0, 544\n"
      "s_nop 0\n"
      "buffer_load_dwordx4 v102, s[40:43], s64 offen offset:3072 nt lds\n"
      "s_add_u32 m0, m0, 544\n"
      "s_nop 0\n"
      "buffer_load_dwordx4 v102, s[40:43], s64 offen offset:3584 nt lds\n"
      "s_waitcnt vmcnt(16)\n"
      "ds_read_b128 a[32:35], v117\n"
      "ds_read_b128 a[36:39], v117 offset:64\n"
      "ds_read_b128 a[40:43], v117 offset:128\n"
      "ds_read_b128 a[44:47], v117 offset:192\n"
      "ds_read_b128 a[48:51], v117 offset:256\n"
      "ds_read_b128 a[52:55], v117 offset:320\n"
      "ds_read_b128 a[56:59], v117 offset:384\n"
      "ds_read_b128 a[60:63], v117 offset:448\n"
      "s_waitcnt lgkmcnt(7)\n"
      "v_mfma_f32_16x16x32_bf16 v[4:7], a[32:35], v[64:67], 0\n"
      "s_waitcnt lgkmcnt(6)\n"
      "v_mfma_f32_16x16x32_bf16 v[4:7], a[36:39], v[68:71], v[4:7]\n"
      "s_waitcnt lgkmcnt(5)\n"
      "v_mfma_f32_16x16x32_bf16 v[4:7], a[40:43], v[72:75], v[4:7]\n"
      "s_waitcnt lgkmcnt(4)\n"
      "v_mfma_f32_16x16x32_bf16 v[4:7], a[44:47], v[76:79], v[4:7]\n"
      "s_waitcnt lgkmcnt(3)\n"
      "v_mfma_f32_16x16x32_bf16 v[4:7], a[48:51], v[80:83], v[4:7]\n"
      "v_max_f32 v118, v0, v1\n"
      "s_waitcnt lgkmcnt(2)\n"
      "v_mfma_f32_16x16x32_bf16 v[4:7], a[52:55], v[84:87], v[4:7]\n"
      "v_max_f32 v119, v24, v25\n"
      "s_waitcnt lgkmcnt(1)\n"
      "v_mfma_f32_16x16x32_bf16 v[4:7], a[56:59], v[88:91], v[4:7]\n"
      "v_max3_f32 v118, v118, v2, v3\n"
      "s_waitcnt lgkmcnt(0)\n"
      "v_mfma_f32_16x16x32_bf16 v[4:7], a[60:63], v[92:95], v[4:7]\n"
      "v_max3_f32 v119, v119, v26, v27\n"
      "v_mfma_f32_16x16x32_bf16 v[28:31], a[32:35], v[128:131], 0\n"
      "v_mfma_f32_16x16x32_bf16 v[28:31], a[36:39], v[132:135], v[28:31]\n"
      "v_mfma_f32_16x16x32_bf16 v[28:31], a[40:43], v[136:139], v[28:31]\n"
      "v_mfma_f32_16x16x32_bf16 v[28:31], a[44:47], v[140:143], v[28:31]\n"
      "v_mfma_f32_16x16x32_bf16 v[28:31], a[48:51], v[144:147], v[28:31]\n"
      "v_mfma_f32_16x16x32_bf16 v[28:31], a[52:55], v[148:151], v[28:31]\n"
      "v_mfma_f32_16x16x32_bf16 v[28:31], a[56:59], v[152:155], v[28:31]\n"
      "v_mfma_f32_16x16x32_bf16 v[28:31], a[60:63], v[156:159], v[28:31]\n"
      "s_add_u32 m0, s53, 0\n"
      "s_add_u32 s64, s65, 32768\n"
      "buffer_load_dwordx4 v102, s[40:43], s64 offen nt lds\n"
      "s_add_u32 m0, m0, 544\n"
      "s_nop 0\n"
      "buffer_load_dwordx4 v102, s[40:43], s64 offen offset:512 nt lds\n"
      "s_add_u32 m0, m0, 544\n"
      "s_nop 0\n"
      "buffer_load_dwordx4 v102, s[40:43], s64 offen offset:1024 nt lds\n"
      "s_add_u32 m0, m0, 544\n"
      "s_nop 0\n"
      "buffer_load_dwordx4 v102, s[40:43], s64 offen offset:1536 nt lds\n"
      "s_add_u32 m0, m0, 544\n"
      "s_nop 0\n"
      "buffer_load_dwordx4 v102, s[40:43], s64 offen offset:2048 nt lds\n"
      "s_add_u32 m0, m0, 544\n"
      "s_nop 0\n"
      "buffer_load_dwordx4 v102, s[40:43], s64 offen offset:2560 nt lds\n"
      "s_add_u32 m0, m0, 544\n"
      "s_nop 0\n"
      "buffer_load_dwordx4 v102, s[40:43], s64 offen offset:3072 nt lds\n"
      "s_add_u32 m0, m0, 544\n"
      "s_nop 0\n"
      "buffer_load_dwordx4 v102, s[40:43], s64 offen offset:3584 nt lds\n"
      "s_waitcnt vmcnt(16)\n"
      "ds_read_b128 a[64:67], v117 offset:8448\n"
      "ds_read_b128 a[68:71], v117 offset:8512\n"
      "ds_read_b128 a[72:75], v117 offset:8576\n"
      "ds_read_b128 a[76:79], v117 offset:8640\n"
      "ds_read_b128 a[80:83], v117 offset:8704\n"
      "ds_read_b128 a[84:87], v117 offset:8768\n"
      "ds_read_b128 a[88:91], v117 offset:8832\n"
      "ds_read_b128 a[92:95], v117 offset:8896\n"
      "s_waitcnt lgkmcnt(7)\n"
      "v_mfma_f32_16x16x32_bf16 v[8:11], a[64:67], v[64:67], 0\n"
      "s_waitcnt lgkmcnt(6)\n"
      "v_mfma_f32_16x16x32_bf16 v[8:11], a[68:71], v[68:71], v[8:11]\n"
      "s_waitcnt lgkmcnt(5)\n"
      "v_mfma_f32_16x16x32_bf16 v[8:11], a[72:75], v[72:75], v[8:11]\n"
      "s_waitcnt lgkmcnt(4)\n"
      "v_mfma_f32_16x16x32_bf16 v[8:11], a[76:79], v[76:79], v[8:11]\n"
      "s_waitcnt lgkmcnt(3)\n"
      "v_mfma_f32_16x16x32_bf16 v[8:11], a[80:83], v[80:83], v[8:11]\n"
      "v_max3_f32 v118, v118, v4, v5\n"
      "s_waitcnt lgkmcnt(2)\n"
      "v_mfma_f32_16x16x32_bf16 v[8:11], a[84:87], v[84:87], v[8:11]\n"
      "v_max3_f32 v119, v119, v28, v29\n"
      "s_waitcnt lgkmcnt(1)\n"
      "v_mfma_f32_16x16x32_bf16 v[8:11], a[88:91], v[88:91], v[8:11]\n"
      "v_max3_f32 v118, v118, v6, v7\n"
      "s_waitcnt lgkmcnt(0)\n"
      "v_mfma_f32_16x16x32_bf16 v[8:11], a[92:95], v[92:95], v[8:11]\n"
      "v_max3_f32 v119, v119, v30, v31\n"
      "v_mfma_f32_16x16x32_bf16 v[32:35], a[64:67], v[128:131], 0\n"
      "v_mfma_f32_16x16x32_bf16 v[32:35], a[68:71], v[132:135], v[32:35]\n"
      "v_mfma_f32_16x16x32_bf16 v[32:35], a[72:75], v[136:139], v[32:35]\n"
      "v_mfma_f32_16x16x32_bf16 v[32:35], a[76:79], v[140:143], v[32:35]\n"
      "v_mfma_f32_16x16x32_bf16 v[32:35], a[80:83], v[144:147], v[32:35]\n"
      "v_mfma_f32_16x16x32_bf16 v[32:35], a[84:87], v[148:151], v[32:35]\n"
      "v_mfma_f32_16x16x32_bf16 v[32:35], a[88:91], v[152:155], v[32:35]\n"
      "v_mfma_f32_16x16x32_bf16 v[32:35], a[92:95], v[156:159], v[32:35]\n"
      "s_add_u32 m0, s53, 8448\n"
      "s_add_u32 s64, s65, 40960\n"
      "buffer_load_dwordx4 v102, s[40:43], s64 offen nt lds\n"
      "s_add_u32 m0, m0, 544\n"
      "s_nop 0\n"
      "buffer_load_dwordx4 v102, s[40:43], s64 offen offset:512 nt lds\n"
      "s_add_u32 m0, m0, 544\n"
      "s_nop 0\n"
      "buffer_load_dwordx4 v102, s[40:43], s64 offen offset:1024 nt lds\n"
      "s_add_u32 m0, m0, 544\n"
      "s_nop 0\n"
      "buffer_load_dwordx4 v102, s[40:43], s64 offen offset:1536 nt lds\n"
      "s_add_u32 m0, m0, 544\n"
      "s_nop 0\n"
      "buffer_load_dwordx4 v102, s[40:43], s64 offen offset:2048 nt lds\n"
      "s_add_u32 m0, m0, 544\n"
      "s_nop 0\n"
      "buffer_load_dwordx4 v102, s[40:43], s64 offen offset:2560 nt lds\n"
      "s_add_u32 m0, m0, 544\n"
      "s_nop 0\n"
      "buffer_load_dwordx4 v102, s[40:43], s64 offen offset:3072 nt lds\n"
      "s_add_u32 m0, m0, 544\n"
      "s_nop 0\n"
      "buffer_load_dwordx4 v102, s[40:43], s64 offen offset:3584 nt lds\n"
      "s_waitcnt vmcnt(16)\n"
      "ds_read_b128 a[96:99], v116\n"
      "ds_read_b128 a[100:103], v116 offset:64\n"
      "ds_read_b128 a[104:107], v116 offset:128\n"
      "ds_read_b128 a[108:111], v116 offset:192\n"
      "ds_read_b128 a[112:115], v116 offset:256\n"
      "ds_read_b128 a[116:119], v116 offset:320\n"
      "ds_read_b128 a[120:123], v116 offset:384\n"
      "ds_read_b128 a[124:127], v116 offset:448\n"
      "s_waitcnt lgkmcnt(7)\n"
      "v_mfma_f32_16x16x32_bf16 v[12:15], a[96:99], v[64:67], 0\n"
      "s_waitcnt lgkmcnt(6)\n"
      "v_mfma_f32_16x16x32_bf16 v[12:15], a[100:103], v[68:71], v[12:15]\n"
      "s_waitcnt lgkmcnt(5)\n"
      "v_mfma_f32_16x16x32_bf16 v[12:15], a[104:107], v[72:75], v[12:15]\n"
      "s_waitcnt lgkmcnt(4)\n"
      "v_mfma_f32_16x16x32_bf16 v[12:15], a[108:111], v[76:79], v[12:15]\n"
      "s_waitcnt lgkmcnt(3)\n"
      "v_mfma_f32_16x16x32_bf16 v[12:15], a[112:115], v[80:83], v[12:15]\n"
      "v_max3_f32 v118, v118, v8, v9\n"
      "s_waitcnt lgkmcnt(2)\n"
      "v_mfma_f32_16x16x32_bf16 v[12:15], a[116:119], v[84:87], v[12:15]\n"
      "v_max3_f32 v119, v119, v32, v33\n"
      "s_waitcnt lgkmcnt(1)\n"
      "v_mfma_f32_16x16x32_bf16 v[12:15], a[120:123], v[88:91], v[12:15]\n"
      "v_max3_f32 v118, v118, v10, v11\n"
      "s_waitcnt lgkmcnt(0)\n"
      "v_mfma_f32_16x16x32_bf16 v[12:15], a[124:127], v[92:95], v[12:15]\n"
      "v_max3_f32 v119, v119, v34, v35\n"
      "v_mfma_f32_16x16x32_bf16 v[36:39], a[96:99], v[128:131], 0\n"
      "v_mfma_f32_16x16x32_bf16 v[36:39], a[100:103], v[132:135], v[36:39]\n"
      "v_mfma_f32_16x16x32_bf16 v[36:39], a[104:107], v[136:139], v[36:39]\n"
      "v_mfma_f32_16x16x32_bf16 v[36:39], a[108:111], v[140:143], v[36:39]\n"
      "v_mfma_f32_16x16x32_bf16 v[36:39], a[112:115], v[144:147], v[36:39]\n"
      "v_mfma_f32_16x16x32_bf16 v[36:39], a[116:119], v[148:151], v[36:39]\n"
      "v_mfma_f32_16x16x32_bf16 v[36:39], a[120:123], v[152:155], v[36:39]\n"
      "v_mfma_f32_16x16x32_bf16 v[36:39], a[124:127], v[156:159], v[36:39]\n"
      "s_add_u32 m0, s53, 21120\n"
      "s_nop 0\n"
      "buffer_load_dwordx4 v102, s[36:39], s57 offen nt lds\n"
      "s_add_u32 m0, m0, 544\n"
      "s_nop 0\n"
      "buffer_load_dwordx4 v102, s[36:39], s57 offen offset:512 nt lds\n"
      "s_add_u32 s57, s57, 8192\n"
      "s_add_u32 m0, s53, 23232\n"
      "s_nop 0\n"
      "buffer_load_dwordx4 v102, s[36:39], s57 offen nt lds\n"
      "s_add_u32 m0, m0, 544\n"
      "s_nop 0\n"
      "buffer_load_dwordx4 v102, s[36:39], s57 offen offset:512 nt lds\n"
      "s_add_u32 s57, s57, 8192\n"
      "s_waitcnt vmcnt(12)\n"
      "ds_read_b128 a[128:131], v117\n"
      "ds_read_b128 a[132:135], v117 offset:64\n"
      "ds_read_b128 a[136:139], v117 offset:128\n"
      "ds_read_b128 a[140:143], v117 offset:192\n"
      "ds_read_b128 a[144:147], v117 offset:256\n"
      "ds_read_b128 a[148:151], v117 offset:320\n"
      "ds_read_b128 a[152:155], v117 offset:384\n"
      "ds_read_b128 a[156:159], v117 offset:448\n"
      "s_waitcnt lgkmcnt(7)\n"
      "v_mfma_f32_16x16x32_bf16 v[16:19], a[128:131], v[64:67], 0\n"
      "s_waitcnt lgkmcnt(6)\n"
      "v_mfma_f32_16x16x32_bf16 v[16:19], a[132:135], v[68:71], v[16:19]\n"
      "s_waitcnt lgkmcnt(5)\n"
      "v_mfma_f32_16x16x32_bf16 v[16:19], a[136:139], v[72:75], v[16:19]\n"
      "s_waitcnt lgkmcnt(4)\n"
      "v_mfma_f32_16x16x32_bf16 v[16:19], a[140:143], v[76:79], v[16:19]\n"
      "s_waitcnt lgkmcnt(3)\n"
      "v_mfma_f32_16x16x32_bf16 v[16:19], a[144:147], v[80:83], v[16:19]\n"
      "v_max3_f32 v118, v118, v12, v13\n"
      "s_waitcnt lgkmcnt(2)\n"
      "v_mfma_f32_16x16x32_bf16 v[16:19], a[148:151], v[84:87], v[16:19]\n"
      "v_max3_f32 v119, v119, v36, v37\n"
      "s_waitcnt lgkmcnt(1)\n"
      "v_mfma_f32_16x16x32_bf16 v[16:19], a[152:155], v[88:91], v[16:19]\n"
      "v_max3_f32 v118, v118, v14, v15\n"
      "s_waitcnt lgkmcnt(0)\n"
      "v_mfma_f32_16x16x32_bf16 v[16:19], a[156:159], v[92:95], v[16:19]\n"
      "v_max3_f32 v119, v119, v38, v39\n"
      "v_mfma_f32_16x16x32_bf16 v[40:43], a[128:131], v[128:131], 0\n"
      "v_mfma_f32_16x16x32_bf16 v[40:43], a[132:135], v[132:135], v[40:43]\n"
      "v_mfma_f32_16x16x32_bf16 v[40:43], a[136:139], v[136:139], v[40:43]\n"
      "v_mfma_f32_16x16x32_bf16 v[40:43], a[140:143], v[140:143], v[40:43]\n"
      "v_mfma_f32_16x16x32_bf16 v[40:43], a[144:147], v[144:147], v[40:43]\n"
      "v_mfma_f32_16x16x32_bf16 v[40:43], a[148:151], v[148:151], v[40:43]\n"
      "v_mfma_f32_16x16x32_bf16 v[40:43], a[152:155], v[152:155], v[40:43]\n"
      "v_mfma_f32_16x16x32_bf16 v[40:43], a[156:159], v[156:159], v[40:43]\n"
      "s_add_u32 m0, s53, 25344\n"
      "s_nop 0\n"
      "buffer_load_dwordx4 v102, s[36:39], s57 offen nt lds\n"
      "s_add_u32 m0, m0, 544\n"
      "s_nop 0\n"
      "buffer_load_dwordx4 v102, s[36:39], s57 offen offset:512 nt lds\n"
      "s_add_u32 s57, s57, 8192\n"
      "s_add_u32 m0, s54, 6144\n"
      "s_nop 0\n"
      "buffer_load_dwordx4 v102, s[36:39], s57 offen nt lds\n"
      "s_add_u32 m0, m0, 544\n"
      "s_nop 0\n"
      "buffer_load_dwordx4 v102, s[36:39], s57 offen offset:512 nt lds\n"
      "s_add_u32 s57, s57, 8192\n"
      "s_waitcnt vmcnt(8)\n"
      "ds_read_b128 a[160:163], v117 offset:8448\n"
      "ds_read_b128 a[164:167], v117 offset:8512\n"
      "ds_read_b128 a[168:171], v117 offset:8576\n"
      "ds_read_b128 a[172:175], v117 offset:8640\n"
      "ds_read_b128 a[176:179], v117 offset:8704\n"
      "ds_read_b128 a[180:183], v117 offset:8768\n"
      "ds_read_b128 a[184:187], v117 offset:8832\n"
      "ds_read_b128 a[188:191], v117 offset:8896\n"
      "s_waitcnt lgkmcnt(7)\n"
      "v_mfma_f32_16x16x32_bf16 v[20:23], a[160:163], v[64:67], 0\n"
      "s_waitcnt lgkmcnt(6)\n"
      "v_mfma_f32_16x16x32_bf16 v[20:23], a[164:167], v[68:71], v[20:23]\n"
      "s_waitcnt lgkmcnt(5)\n"
      "v_mfma_f32_16x16x32_bf16 v[20:23], a[168:171], v[72:75], v[20:23]\n"
      "s_waitcnt lgkmcnt(4)\n"
      "v_mfma_f32_16x16x32_bf16 v[20:23], a[172:175], v[76:79], v[20:23]\n"
      "s_waitcnt lgkmcnt(3)\n"
      "v_mfma_f32_16x16x32_bf16 v[20:23], a[176:179], v[80:83], v[20:23]\n"
      "v_max3_f32 v118, v118, v16, v17\n"
      "s_waitcnt lgkmcnt(2)\n"
      "v_mfma_f32_16x16x32_bf16 v[20:23], a[180:183], v[84:87], v[20:23]\n"
      "v_max3_f32 v119, v119, v40, v41\n"
      "s_waitcnt lgkmcnt(1)\n"
      "v_mfma_f32_16x16x32_bf16 v[20:23], a[184:187], v[88:91], v[20:23]\n"
      "v_max3_f32 v118, v118, v18, v19\n"
      "s_waitcnt lgkmcnt(0)\n"
      "v_mfma_f32_16x16x32_bf16 v[20:23], a[188:191], v[92:95], v[20:23]\n"
      "v_max3_f32 v119, v119, v42, v43\n"
      "v_mfma_f32_16x16x32_bf16 v[44:47], a[160:163], v[128:131], 0\n"
      "v_mfma_f32_16x16x32_bf16 v[44:47], a[164:167], v[132:135], v[44:47]\n"
      "v_mfma_f32_16x16x32_bf16 v[44:47], a[168:171], v[136:139], v[44:47]\n"
      "v_mfma_f32_16x16x32_bf16 v[44:47], a[172:175], v[140:143], v[44:47]\n"
      "v_mfma_f32_16x16x32_bf16 v[44:47], a[176:179], v[144:147], v[44:47]\n"
      "v_mfma_f32_16x16x32_bf16 v[44:47], a[180:183], v[148:151], v[44:47]\n"
      "v_mfma_f32_16x16x32_bf16 v[44:47], a[184:187], v[152:155], v[44:47]\n"
      "v_mfma_f32_16x16x32_bf16 v[44:47], a[188:191], v[156:159], v[44:47]\n"
      "s_add_u32 m0, s53, 0\n"
      "s_mov_b32 s64, 0\n"
      "buffer_load_dwordx4 v113, s[44:47], s64 offen nt lds\n"
      "buffer_load_dwordx4 v112, s[44:47], s64 offen offset:1024 nt lds\n"
      "buffer_load_dwordx4 v113, s[44:47], s64 offen offset:2048 nt lds\n"
      "buffer_load_dwordx4 v112, s[44:47], s64 offen offset:3072 nt lds\n"
      "v_max3_f32 v118, v118, v20, v21\n"
      "s_nop 0\n"
      "v_max3_f32 v119, v119, v44, v45\n"
      "v_max3_f32 v118, v118, v22, v23\n"
      "v_max3_f32 v119, v119, v46, v47\n"
      "ds_max_f32 v104, v118 offset:128\n"
      "ds_max_f32 v104, v119 offset:192\n"
      "s_mul_i32 s63, s52, 3072\n"
      "s_add_u32 s63, s63, 147712\n"
      "v_add_u32 v124, s63, v98\n"
      "s_add_u32 s63, s53, 16896\n"
      "v_add_u32 v125, s63, v98\n"
      "s_add_u32 m0, s53, 4224\n"
      "s_mov_b32 s64, 16384\n"
      "buffer_load_dwordx4 v113, s[44:47], s64 offen nt lds\n"
      "buffer_load_dwordx4 v112, s[44:47], s64 offen offset:1024 nt lds\n"
      "buffer_load_dwordx4 v113, s[44:47], s64 offen offset:2048 nt lds\n"
      "buffer_load_dwordx4 v112, s[44:47], s64 offen offset:3072 nt lds\n"
      "v_add_u32 v96, 21120, v97\n"
      "s_waitcnt vmcnt(12) lgkmcnt(0)\n"
      "s_barrier\n"
      "ds_read_b32 v118, v104 offset:128\n"
      "ds_read_b32 v119, v104 offset:192\n"
      "ds_read_b128 v[64:67], v96\n"
      "ds_read_b128 v[68:71], v96 offset:64\n"
      "ds_read_b128 v[72:75], v96 offset:128\n"
      "ds_read_b128 v[76:79], v96 offset:192\n"
      "s_waitcnt lgkmcnt(3)\n"
      "v_mfma_f32_16x16x32_bf16 v[184:187], a[0:3], v[64:67], 0\n"
      "v_fma_f32 v105, v118, s56, v111\n"
      "s_waitcnt lgkmcnt(2)\n"
      "v_mfma_f32_16x16x32_bf16 v[184:187], a[4:7], v[68:71], v[184:187]\n"
      "ds_read_b128 v[80:83], v96 offset:256\n"
      "v_fma_f32 v0, v0, s55, v105\n"
      "v_mfma_f32_16x16x32_bf16 v[188:191], a[36:39], v[68:71], 0\n"
      "v_fma_f32 v1, v1, s55, v105\n"
      "v_fma_f32 v2, v2, s55, v105\n"
      "v_mfma_f32_16x16x32_bf16 v[188:191], a[32:35], v[64:67], v[188:191]\n"
      "ds_read_b128 v[84:87], v96 offset:320\n"
      "s_add_u32 m0, s53, 8448\n"
      "s_mov_b32 s64, 32768\n"
      "v_mfma_f32_16x16x32_bf16 v[192:195], a[64:67], v[64:67], 0\n"
      "buffer_load_dwordx4 v113, s[44:47], s64 offen nt lds\n"
      "v_fma_f32 v3, v3, s55, v105\n"
      "v_mfma_f32_16x16x32_bf16 v[192:195], a[68:71], v[68:71], v[192:195]\n"
      "ds_read_b128 v[88:91], v96 offset:384\n"
      "v_fma_f32 v4, v4, s55, v105\n"
      "v_mfma_f32_16x16x32_bf16 v[196:199], a[100:103], v[68:71], 0\n"
      "v_exp_f32 v0, v0\n"
      "v_mfma_f32_16x16x32_bf16 v[196:199], a[96:99], v[64:67], v[196:199]\n"
      "ds_read_b128 v[92:95], v96 offset:448\n"
      "v_fma_f32 v5, v5, s55, v105\n"
      "v_mfma_f32_16x16x32_bf16 v[200:203], a[128:131], v[64:67], 0\n"
      "v_exp_f32 v1, v1\n"
      "v_mfma_f32_16x16x32_bf16 v[200:203], a[132:135], v[68:71], v[200:203]\n"
      "v_exp_f32 v2, v2\n"
      "v_mfma_f32_16x16x32_bf16 v[204:207], a[164:167], v[68:71], 0\n"
      "v_add_u32 v96, 23232, v97\n"
      "v_add_f32 v106, v0, v1\n"
      "v_mfma_f32_16x16x32_bf16 v[204:207], a[160:163], v[64:67], v[204:207]\n"
      "v_cvt_pk_bf16_f32 v0, v0, v1\n"
      "v_add_f32 v106, v106, v2\n"
      "s_waitcnt lgkmcnt(5)\n"
      "v_mfma_f32_16x16x32_bf16 v[184:187], a[8:11], v[72:75], v[184:187]\n"
      "buffer_load_dwordx4 v112, s[44:47], s64 offen offset:1024 nt lds\n"
      "v_fma_f32 v6, v6, s55, v105\n"
      "s_waitcnt lgkmcnt(4)\n"
      "v_mfma_f32_16x16x32_bf16 v[184:187], a[12:15], v[76:79], v[184:187]\n"
      "ds_read_b128 v[64:67], v96\n"
      "v_fma_f32 v7, v7, s55, v105\n"
      "v_mfma_f32_16x16x32_bf16 v[188:191], a[44:47], v[76:79], v[188:191]\n"
      "v_exp_f32 v3, v3\n"
      "v_mfma_f32_16x16x32_bf16 v[188:191], a[40:43], v[72:75], v[188:191]\n"
      "ds_read_b128 v[68:71], v96 offset:64\n"
      "v_fma_f32 v8, v8, s55, v105\n"
      "v_mfma_f32_16x16x32_bf16 v[192:195], a[72:75], v[72:75], v[192:195]\n"
      "v_add_f32 v106, v106, v3\n"
      "v_cvt_pk_bf16_f32 v1, v2, v3\n"
      "v_mfma_f32_16x16x32_bf16 v[192:195], a[76:79], v[76:79], v[192:195]\n"
      "v_exp_f32 v4, v4\n"
      "v_mfma_f32_16x16x32_bf16 v[196:199], a[108:111], v[76:79], v[196:199]\n"
      "v_exp_f32 v5, v5\n"
      "v_mfma_f32_16x16x32_bf16 v[196:199], a[104:107], v[72:75], v[196:199]\n"
      "v_add_f32 v106, v106, v4\n"
      "v_fma_f32 v9, v9, s55, v105\n"
      "v_mfma_f32_16x16x32_bf16 v[200:203], a[136:139], v[72:75], v[200:203]\n"
      "buffer_load_dwordx4 v113, s[44:47], s64 offen offset:2048 nt lds\n"
      "v_add_f32 v106, v106, v5\n"
      "v_mfma_f32_16x16x32_bf16 v[200:203], a[140:143], v[76:79], v[200:203]\n"
      "v_cvt_pk_bf16_f32 v2, v4, v5\n"
      "v_fma_f32 v10, v10, s55, v105\n"
      "v_mfma_f32_16x16x32_bf16 v[204:207], a[172:175], v[76:79], v[204:207]\n"
      "v_exp_f32 v6, v6\n"
      "v_mfma_f32_16x16x32_bf16 v[204:207], a[168:171], v[72:75], v[204:207]\n"
      "v_exp_f32 v7, v7\n"
      "s_waitcnt lgkmcnt(5)\n"
      "v_mfma_f32_16x16x32_bf16 v[184:187], a[16:19], v[80:83], v[184:187]\n"
      "v_add_f32 v106, v106, v6\n"
      "v_fma_f32 v11, v11, s55, v105\n"
      "s_waitcnt lgkmcnt(4)\n"
      "v_mfma_f32_16x16x32_bf16 v[184:187], a[20:23], v[84:87], v[184:187]\n"
      "ds_read_b128 v[72:75], v96 offset:128\n"
      "v_add_f32 v106, v106, v7\n"
      "v_mfma_f32_16x16x32_bf16 v[188:191], a[52:55], v[84:87], v[188:191]\n"
      "v_cvt_pk_bf16_f32 v3, v6, v7\n"
      "v_fma_f32 v12, v12, s55, v105\n"
      "v_mfma_f32_16x16x32_bf16 v[188:191], a[48:51], v[80:83], v[188:191]\n"
      "ds_read_b128 v[76:79], v96 offset:192\n"
      "v_fma_f32 v13, v13, s55, v105\n"
      "v_mfma_f32_16x16x32_bf16 v[192:195], a[80:83], v[80:83], v[192:195]\n"
      "buffer_load_dwordx4 v112, s[44:47], s64 offen offset:3072 nt lds\n"
      "v_fma_f32 v14, v14, s55, v105\n"
      "v_mfma_f32_16x16x32_bf16 v[192:195], a[84:87], v[84:87], v[192:195]\n"
      "ds_write_b128 v124, v[0:3]\n"
      "v_mfma_f32_16x16x32_bf16 v[196:199], a[116:119], v[84:87], v[196:199]\n"
      "v_exp_f32 v8, v8\n"
      "v_mfma_f32_16x16x32_bf16 v[196:199], a[112:115], v[80:83], v[196:199]\n"
      "v_exp_f32 v9, v9\n"
      "v_mfma_f32_16x16x32_bf16 v[200:203], a[144:147], v[80:83], v[200:203]\n"
      "v_add_f32 v106, v106, v8\n"
      "v_fma_f32 v15, v15, s55, v105\n"
      "v_mfma_f32_16x16x32_bf16 v[200:203], a[148:151], v[84:87], v[200:203]\n"
      "v_add_f32 v106, v106, v9\n"
      "v_cvt_pk_bf16_f32 v8, v8, v9\n"
      "v_mfma_f32_16x16x32_bf16 v[204:207], a[180:183], v[84:87], v[204:207]\n"
      "v_exp_f32 v10, v10\n"
      "v_mfma_f32_16x16x32_bf16 v[204:207], a[176:179], v[80:83], v[204:207]\n"
      "v_exp_f32 v11, v11\n"
      "s_waitcnt lgkmcnt(6)\n"
      "v_mfma_f32_16x16x32_bf16 v[184:187], a[24:27], v[88:91], v[184:187]\n"
      "v_add_f32 v106, v106, v10\n"
      "v_fma_f32 v16, v16, s55, v105\n"
      "s_waitcnt lgkmcnt(5)\n"
      "v_mfma_f32_16x16x32_bf16 v[184:187], a[28:31], v[92:95], v[184:187]\n"
      "ds_read_b128 v[80:83], v96 offset:256\n"
      "v_add_f32 v106, v106, v11\n"
      "v_mfma_f32_16x16x32_bf16 v[188:191], a[60:63], v[92:95], v[188:191]\n"
      "v_cvt_pk_bf16_f32 v9, v10, v11\n"
      "v_fma_f32 v17, v17, s55, v105\n"
      "v_mfma_f32_16x16x32_bf16 v[188:191], a[56:59], v[88:91], v[188:191]\n"
      "s_add_u32 m0, s53, 12672\n"
      "s_mov_b32 s64, 49152\n"
      "v_fma_f32 v18, v18, s55, v105\n"
      "v_mfma_f32_16x16x32_bf16 v[192:195], a[88:91], v[88:91], v[192:195]\n"
      "buffer_load_dwordx4 v113, s[44:47], s64 offen nt lds\n"
      "v_fma_f32 v19, v19, s55, v105\n"
      "v_mfma_f32_16x16x32_bf16 v[192:195], a[92:95], v[92:95], v[192:195]\n"
      "ds_read_b128 v[84:87], v96 offset:320\n"
      "v_max3_f32 v108, v184, v185, v186\n"
      "v_mfma_f32_16x16x32_bf16 v[196:199], a[124:127], v[92:95], v[196:199]\n"
      "v_exp_f32 v12, v12\n"
      "v_mfma_f32_16x16x32_bf16 v[196:199], a[120:123], v[88:91], v[196:199]\n"
      "v_max3_f32 v108, v108, v187, v188\n"
      "v_max3_f32 v108, v108, v189, v190\n"
      "v_mfma_f32_16x16x32_bf16 v[200:203], a[152:155], v[88:91], v[200:203]\n"
      "v_add_f32 v106, v106, v12\n"
      "v_fma_f32 v20, v20, s55, v105\n"
      "v_mfma_f32_16x16x32_bf16 v[200:203], a[156:159], v[92:95], v[200:203]\n"
      "v_max3_f32 v108, v108, v191, v192\n"
      "v_max3_f32 v108, v108, v193, v194\n"
      "v_mfma_f32_16x16x32_bf16 v[204:207], a[188:191], v[92:95], v[204:207]\n"
      "v_exp_f32 v13, v13\n"
      "v_mfma_f32_16x16x32_bf16 v[204:207], a[184:187], v[88:91], v[204:207]\n"
      "buffer_load_dwordx4 v112, s[44:47], s64 offen offset:1024 nt lds\n"
      "v_max3_f32 v108, v108, v195, v196\n"
      "s_waitcnt lgkmcnt(6)\n"
      "v_mfma_f32_16x16x32_bf16 v[208:211], a[0:3], v[64:67], 0\n"
      "v_max3_f32 v108, v108, v197, v198\n"
      "v_add_f32 v106, v106, v13\n"
      "s_waitcnt lgkmcnt(5)\n"
      "v_mfma_f32_16x16x32_bf16 v[208:211], a[4:7], v[68:71], v[208:211]\n"
      "ds_read_b128 v[88:91], v96 offset:384\n"
      "v_max3_f32 v108, v108, v199, v200\n"
      "v_mfma_f32_16x16x32_bf16 v[212:215], a[36:39], v[68:71], 0\n"
      "v_max3_f32 v108, v108, v201, v202\n"
      "v_cvt_pk_bf16_f32 v10, v12, v13\n"
      "v_mfma_f32_16x16x32_bf16 v[212:215], a[32:35], v[64:67], v[212:215]\n"
      "ds_read_b128 v[92:95], v96 offset:448\n"
      "v_max3_f32 v108, v108, v203, v204\n"
      "v_mfma_f32_16x16x32_bf16 v[216:219], a[64:67], v[64:67], 0\n"
      "v_max3_f32 v108, v108, v205, v206\n"
      "v_max_f32 v108, v108, v207\n"
      "v_mfma_f32_16x16x32_bf16 v[216:219], a[68:71], v[68:71], v[216:219]\n"
      "ds_max_f32 v104, v108 offset:256\n"
      "v_fma_f32 v21, v21, s55, v105\n"
      "v_mfma_f32_16x16x32_bf16 v[220:223], a[100:103], v[68:71], 0\n"
      "v_exp_f32 v14, v14\n"
      "v_mfma_f32_16x16x32_bf16 v[220:223], a[96:99], v[64:67], v[220:223]\n"
      "buffer_load_dwordx4 v113, s[44:47], s64 offen offset:2048 nt lds\n"
      "v_fma_f32 v22, v22, s55, v105\n"
      "v_mfma_f32_16x16x32_bf16 v[224:227], a[128:131], v[64:67], 0\n"
      "v_add_f32 v106, v106, v14\n"
      "v_fma_f32 v23, v23, s55, v105\n"
      "v_mfma_f32_16x16x32_bf16 v[224:227], a[132:135], v[68:71], v[224:227]\n"
      "v_fma_f32 v105, v119, s56, v111\n"
      "v_mfma_f32_16x16x32_bf16 v[228:231], a[164:167], v[68:71], 0\n"
      "v_exp_f32 v15, v15\n"
      "v_mfma_f32_16x16x32_bf16 v[228:231], a[160:163], v[64:67], v[228:231]\n"
      "v_exp_f32 v16, v16\n"
      "s_waitcnt lgkmcnt(7)\n"
      "v_mfma_f32_16x16x32_bf16 v[208:211], a[8:11], v[72:75], v[208:211]\n"
      "v_add_f32 v106, v106, v15\n"
      "v_cvt_pk_bf16_f32 v11, v14, v15\n"
      "s_waitcnt lgkmcnt(6)\n"
      "v_mfma_f32_16x16x32_bf16 v[208:211], a[12:15], v[76:79], v[208:211]\n"
      "v_add_f32 v106, v106, v16\n"
      "v_fma_f32 v24, v24, s55, v105\n"
      "v_mfma_f32_16x16x32_bf16 v[212:215], a[44:47], v[76:79], v[212:215]\n"
      "buffer_load_dwordx4 v112, s[44:47], s64 offen offset:3072 nt lds\n"
      "v_fma_f32 v25, v25, s55, v105\n"
      "v_mfma_f32_16x16x32_bf16 v[212:215], a[40:43], v[72:75], v[212:215]\n"
      "ds_write_b128 v124, v[8:11] offset:1024\n"
      "v_mfma_f32_16x16x32_bf16 v[216:219], a[72:75], v[72:75], v[216:219]\n"
      "v_exp_f32 v17, v17\n"
      "v_mfma_f32_16x16x32_bf16 v[216:219], a[76:79], v[76:79], v[216:219]\n"
      "v_exp_f32 v18, v18\n"
      "v_mfma_f32_16x16x32_bf16 v[220:223], a[108:111], v[76:79], v[220:223]\n"
      "v_add_f32 v106, v106, v17\n"
      "v_cvt_pk_bf16_f32 v16, v16, v17\n"
      "v_mfma_f32_16x16x32_bf16 v[220:223], a[104:107], v[72:75], v[220:223]\n"
      "v_add_f32 v106, v106, v18\n"
      "v_fma_f32 v26, v26, s55, v105\n"
      "v_mfma_f32_16x16x32_bf16 v[224:227], a[136:139], v[72:75], v[224:227]\n"
      "v_exp_f32 v19, v19\n"
      "v_mfma_f32_16x16x32_bf16 v[224:227], a[140:143], v[76:79], v[224:227]\n"
      "v_exp_f32 v20, v20\n"
      "v_mfma_f32_16x16x32_bf16 v[228:231], a[172:175], v[76:79], v[228:231]\n"
      "v_add_f32 v106, v106, v19\n"
      "v_cvt_pk_bf16_f32 v17, v18, v19\n"
      "v_mfma_f32_16x16x32_bf16 v[228:231], a[168:171], v[72:75], v[228:231]\n"
      "v_add_f32 v106, v106, v20\n"
      "v_fma_f32 v27, v27, s55, v105\n"
      "s_waitcnt lgkmcnt(5)\n"
      "v_mfma_f32_16x16x32_bf16 v[208:211], a[16:19], v[80:83], v[208:211]\n"
      "v_exp_f32 v21, v21\n"
      "s_waitcnt lgkmcnt(4)\n"
      "v_mfma_f32_16x16x32_bf16 v[208:211], a[20:23], v[84:87], v[208:211]\n"
      "v_exp_f32 v24, v24\n"
      "v_mfma_f32_16x16x32_bf16 v[212:215], a[52:55], v[84:87], v[212:215]\n"
      "v_add_f32 v106, v106, v21\n"
      "v_cvt_pk_bf16_f32 v18, v20, v21\n"
      "v_mfma_f32_16x16x32_bf16 v[212:215], a[48:51], v[80:83], v[212:215]\n"
      "v_exp_f32 v22, v22\n"
      "v_mfma_f32_16x16x32_bf16 v[216:219], a[80:83], v[80:83], v[216:219]\n"
      "s_waitcnt vmcnt(12)\n"
      "v_exp_f32 v25, v25\n"
      "v_mfma_f32_16x16x32_bf16 v[216:219], a[84:87], v[84:87], v[216:219]\n"
      "ds_read_b64_tr_b16 a[192:193], v114\n"
      "v_add_f32 v106, v106, v22\n"
      "v_mfma_f32_16x16x32_bf16 v[220:223], a[116:119], v[84:87], v[220:223]\n"
      "ds_read_b64_tr_b16 a[194:195], v114 offset:512\n"
      "v_fma_f32 v28, v28, s55, v105\n"
      "v_mfma_f32_16x16x32_bf16 v[220:223], a[112:115], v[80:83], v[220:223]\n"
      "ds_read_b64_tr_b16 a[196:197], v114 offset:8\n"
      "v_fma_f32 v29, v29, s55, v105\n"
      "v_mfma_f32_16x16x32_bf16 v[224:227], a[144:147], v[80:83], v[224:227]\n"
      "ds_read_b64_tr_b16 a[198:199], v114 offset:520\n"
      "v_fma_f32 v30, v30, s55, v105\n"
      "v_mfma_f32_16x16x32_bf16 v[224:227], a[148:151], v[84:87], v[224:227]\n"
      "ds_read_b64_tr_b16 a[200:201], v115\n"
      "v_fma_f32 v31, v31, s55, v105\n"
      "v_mfma_f32_16x16x32_bf16 v[228:231], a[180:183], v[84:87], v[228:231]\n"
      "ds_read_b64_tr_b16 a[202:203], v115 offset:512\n"
      "v_fma_f32 v32, v32, s55, v105\n"
      "v_mfma_f32_16x16x32_bf16 v[228:231], a[176:179], v[80:83], v[228:231]\n"
      "ds_read_b64_tr_b16 a[204:205], v115 offset:8\n"
      "v_fma_f32 v33, v33, s55, v105\n"
      "s_waitcnt lgkmcnt(10)\n"
      "v_mfma_f32_16x16x32_bf16 v[208:211], a[24:27], v[88:91], v[208:211]\n"
      "ds_read_b64_tr_b16 a[206:207], v115 offset:520\n"
      "v_fma_f32 v34, v34, s55, v105\n"
      "s_waitcnt lgkmcnt(10)\n"
      "v_mfma_f32_16x16x32_bf16 v[208:211], a[28:31], v[92:95], v[208:211]\n"
      "v_exp_f32 v23, v23\n"
      "v_mfma_f32_16x16x32_bf16 v[212:215], a[60:63], v[92:95], v[212:215]\n"
      "v_exp_f32 v26, v26\n"
      "v_mfma_f32_16x16x32_bf16 v[212:215], a[56:59], v[88:91], v[212:215]\n"
      "v_add_f32 v106, v106, v23\n"
      "v_cvt_pk_bf16_f32 v19, v22, v23\n"
      "v_mfma_f32_16x16x32_bf16 v[216:219], a[88:91], v[88:91], v[216:219]\n"
      "s_add_u32 m0, s53, 0\n"
      "s_mov_b32 s64, 65536\n"
      "v_fma_f32 v35, v35, s55, v105\n"
      "v_mfma_f32_16x16x32_bf16 v[216:219], a[92:95], v[92:95], v[216:219]\n"
      "s_waitcnt lgkmcnt(0)\n"
      "buffer_load_dwordx4 v113, s[44:47], s64 offen nt lds\n"
      "v_max3_f32 v109, v208, v209, v210\n"
      "v_mfma_f32_16x16x32_bf16 v[220:223], a[124:127], v[92:95], v[220:223]\n"
      "ds_write_b128 v124, v[16:19] offset:2048\n"
      "v_mfma_f32_16x16x32_bf16 v[220:223], a[120:123], v[88:91], v[220:223]\n"
      "v_max3_f32 v109, v109, v211, v212\n"
      "v_max3_f32 v109, v109, v213, v214\n"
      "v_mfma_f32_16x16x32_bf16 v[224:227], a[152:155], v[88:91], v[224:227]\n"
      "v_cvt_u32_f32 v110, v106\n"
      "v_add_f32 v106, v24, v25\n"
      "v_mfma_f32_16x16x32_bf16 v[224:227], a[156:159], v[92:95], v[224:227]\n"
      "v_max3_f32 v109, v109, v215, v216\n"
      "v_max3_f32 v109, v109, v217, v218\n"
      "v_mfma_f32_16x16x32_bf16 v[228:231], a[188:191], v[92:95], v[228:231]\n"
      "buffer_load_dwordx4 v112, s[44:47], s64 offen offset:1024 nt lds\n"
      "v_cvt_pk_bf16_f32 v24, v24, v25\n"
      "v_mfma_f32_16x16x32_bf16 v[228:231], a[184:187], v[88:91], v[228:231]\n"
      "ds_add_u32 v104, v110 offset:1664\n"
      "v_max3_f32 v109, v109, v219, v220\n"
      "v_max3_f32 v109, v109, v221, v222\n"
      "v_add_f32 v106, v106, v26\n"
      "v_max3_f32 v109, v109, v223, v224\n"
      "v_max3_f32 v109, v109, v225, v226\n"
      "v_exp_f32 v27, v27\n"
      "buffer_load_dwordx4 v113, s[44:47], s64 offen offset:2048 nt lds\n"
      "v_max3_f32 v109, v109, v227, v228\n"
      "v_max3_f32 v109, v109, v229, v230\n"
      "v_max_f32 v109, v109, v231\n"
      "ds_max_f32 v104, v109 offset:320\n"
      "v_add_f32 v106, v106, v27\n"
      "v_cvt_pk_bf16_f32 v25, v26, v27\n"
      "v_fma_f32 v36, v36, s55, v105\n"
      "v_exp_f32 v28, v28\n"
      "buffer_load_dwordx4 v112, s[44:47], s64 offen offset:3072 nt lds\n"
      "v_fma_f32 v37, v37, s55, v105\n"
      "v_add_f32 v106, v106, v28\n"
      "v_fma_f32 v38, v38, s55, v105\n"
      "v_exp_f32 v29, v29\n"
      "v_exp_f32 v30, v30\n"
      "v_add_f32 v106, v106, v29\n"
      "v_cvt_pk_bf16_f32 v26, v28, v29\n"
      "v_add_f32 v106, v106, v30\n"
      "v_fma_f32 v39, v39, s55, v105\n"
      "v_exp_f32 v31, v31\n"
      "v_exp_f32 v32, v32\n"
      "s_waitcnt vmcnt(12)\n"
      "v_add_f32 v106, v106, v31\n"
      "v_cvt_pk_bf16_f32 v27, v30, v31\n"
      "ds_read_b64_tr_b16 a[208:209], v114 offset:4224\n"
      "v_add_f32 v106, v106, v32\n"
      "ds_read_b64_tr_b16 a[210:211], v114 offset:4736\n"
      "v_fma_f32 v40, v40, s55, v105\n"
      "ds_read_b64_tr_b16 a[212:213], v114 offset:4232\n"
      "v_fma_f32 v41, v41, s55, v105\n"
      "ds_read_b64_tr_b16 a[214:215], v114 offset:4744\n"
      "v_fma_f32 v42, v42, s55, v105\n"
      "ds_read_b64_tr_b16 a[216:217], v115 offset:4224\n"
      "v_fma_f32 v43, v43, s55, v105\n"
      "ds_read_b64_tr_b16 a[218:219], v115 offset:4736\n"
      "v_fma_f32 v44, v44, s55, v105\n"
      "ds_read_b64_tr_b16 a[220:221], v115 offset:4232\n"
      "v_fma_f32 v45, v45, s55, v105\n"
      "ds_read_b64_tr_b16 a[222:223], v115 offset:4744\n"
      "v_fma_f32 v46, v46, s55, v105\n"
      "ds_write_b128 v125, v[24:27]\n"
      "v_exp_f32 v33, v33\n"
      "v_exp_f32 v34, v34\n"
      "s_add_u32 m0, s53, 4224\n"
      "s_mov_b32 s64, 81920\n"
      "v_add_f32 v106, v106, v33\n"
      "s_waitcnt lgkmcnt(1)\n"
      "buffer_load_dwordx4 v113, s[44:47], s64 offen nt lds\n"
      "v_cvt_pk_bf16_f32 v32, v32, v33\n"
      "v_add_f32 v106, v106, v34\n"
      "v_fma_f32 v47, v47, s55, v105\n"
      "v_exp_f32 v35, v35\n"
      "v_exp_f32 v36, v36\n"
      "v_add_f32 v106, v106, v35\n"
      "v_cvt_pk_bf16_f32 v33, v34, v35\n"
      "v_add_f32 v106, v106, v36\n"
      "buffer_load_dwordx4 v112, s[44:47], s64 offen offset:1024 nt lds\n"
      "v_exp_f32 v37, v37\n"
      "v_exp_f32 v38, v38\n"
      "v_add_f32 v106, v106, v37\n"
      "v_cvt_pk_bf16_f32 v34, v36, v37\n"
      "v_add_f32 v106, v106, v38\n"
      "v_exp_f32 v39, v39\n"
      "buffer_load_dwordx4 v113, s[44:47], s64 offen offset:2048 nt lds\n"
      "v_add_f32 v106, v106, v39\n"
      "v_cvt_pk_bf16_f32 v35, v38, v39\n"
      "v_exp_f32 v40, v40\n"
      "ds_write_b128 v125, v[32:35] offset:1024\n"
      "v_add_f32 v106, v106, v40\n"
      "v_exp_f32 v41, v41\n"
      "buffer_load_dwordx4 v112, s[44:47], s64 offen offset:3072 nt lds\n"
      "v_add_f32 v106, v106, v41\n"
      "v_cvt_pk_bf16_f32 v40, v40, v41\n"
      "v_exp_f32 v42, v42\n"
      "v_exp_f32 v43, v43\n"
      "v_add_f32 v106, v106, v42\n"
      "v_add_f32 v106, v106, v43\n"
      "v_cvt_pk_bf16_f32 v41, v42, v43\n"
      "v_exp_f32 v44, v44\n"
      "v_exp_f32 v45, v45\n"
      "v_add_f32 v106, v106, v44\n"
      "v_add_f32 v106, v106, v45\n"
      "v_cvt_pk_bf16_f32 v42, v44, v45\n"
      "v_exp_f32 v46, v46\n"
      "v_exp_f32 v47, v47\n"
      "v_add_f32 v106, v106, v46\n"
      "v_add_f32 v106, v106, v47\n"
      "v_cvt_pk_bf16_f32 v43, v46, v47\n"
      "ds_write_b128 v125, v[40:43] offset:2048\n"
      "v_cvt_u32_f32 v110, v106\n"
      "ds_add_u32 v104, v110 offset:1728\n"
      "v_add_u32 v96, 25344, v97\n"
      "s_waitcnt lgkmcnt(0)\n"
      "s_barrier\n"
      "ds_read_b32 v118, v104 offset:256\n"
      "ds_read_b32 v119, v104 offset:320\n"
      "ds_read_b128 v[64:67], v96\n"
      "ds_read_b128 v[68:71], v96 offset:64\n"
      "ds_read_b128 v[72:75], v96 offset:128\n"
      "ds_read_b128 v[76:79], v96 offset:192\n"
      "s_waitcnt lgkmcnt(3)\n"
      "v_mfma_f32_16x16x32_bf16 v[232:235], a[0:3], v[64:67], 0\n"
      "s_waitcnt vmcnt(12)\n"
      "v_fma_f32 v105, v118, s56, v111\n"
      "s_waitcnt lgkmcnt(2)\n"
      "v_mfma_f32_16x16x32_bf16 v[232:235], a[4:7], v[68:71], v[232:235]\n"
      "ds_read_b128 v[80:83], v96 offset:256\n"
      "ds_read_b64_tr_b16 a[224:225], v114 offset:8448\n"
      "v_mfma_f32_16x16x32_bf16 v[236:239], a[36:39], v[68:71], 0\n"
      "ds_read_b64_tr_b16 a[226:227], v114 offset:8960\n"
      "v_fma_f32 v184, v184, s55, v105\n"
      "v_mfma_f32_16x16x32_bf16 v[236:239], a[32:35], v[64:67], v[236:239]\n"
      "ds_read_b128 v[84:87], v96 offset:320\n"
      "ds_read_b64_tr_b16 a[228:229], v114 offset:8456\n"
      "v_mfma_f32_16x16x32_bf16 v[240:243], a[64:67], v[64:67], 0\n"
      "ds_read_b64_tr_b16 a[230:231], v114 offset:8968\n"
      "v_fma_f32 v185, v185, s55, v105\n"
      "v_mfma_f32_16x16x32_bf16 v[240:243], a[68:71], v[68:71], v[240:243]\n"
      "ds_read_b128 v[88:91], v96 offset:384\n"
      "ds_read_b64_tr_b16 a[232:233], v115 offset:8448\n"
      "v_mfma_f32_16x16x32_bf16 v[244:247], a[100:103], v[68:71], 0\n"
      "ds_read_b64_tr_b16 a[234:235], v115 offset:8960\n"
      "v_fma_f32 v186, v186, s55, v105\n"
      "v_mfma_f32_16x16x32_bf16 v[244:247], a[96:99], v[64:67], v[244:247]\n"
      "ds_read_b128 v[92:95], v96 offset:448\n"
      "ds_read_b64_tr_b16 a[236:237], v115 offset:8456\n"
      "v_mfma_f32_16x16x32_bf16 v[248:251], a[128:131], v[64:67], 0\n"
      "ds_read_b64_tr_b16 a[238:239], v115 offset:8968\n"
      "v_fma_f32 v187, v187, s55, v105\n"
      "v_mfma_f32_16x16x32_bf16 v[248:251], a[132:135], v[68:71], v[248:251]\n"
      "v_exp_f32 v184, v184\n"
      "v_mfma_f32_16x16x32_bf16 v[252:255], a[164:167], v[68:71], 0\n"
      "v_mov_b32 v96, v127\n"
      "v_fma_f32 v188, v188, s55, v105\n"
      "v_mfma_f32_16x16x32_bf16 v[252:255], a[160:163], v[64:67], v[252:255]\n"
      "v_exp_f32 v185, v185\n"
      "s_waitcnt lgkmcnt(13)\n"
      "v_mfma_f32_16x16x32_bf16 v[232:235], a[8:11], v[72:75], v[232:235]\n"
      "s_add_u32 m0, s53, 8448\n"
      "s_mov_b32 s64, 98304\n"
      "v_fma_f32 v189, v189, s55, v105\n"
      "s_waitcnt lgkmcnt(12)\n"
      "v_mfma_f32_16x16x32_bf16 v[232:235], a[12:15], v[76:79], v[232:235]\n"
      "s_waitcnt lgkmcnt(0)\n"
      "buffer_load_dwordx4 v113, s[44:47], s64 offen nt lds\n"
      "v_add_f32 v106, v184, v185\n"
      "v_mfma_f32_16x16x32_bf16 v[236:239], a[44:47], v[76:79], v[236:239]\n"
      "ds_read_b128 v[64:67], v96\n"
      "v_cvt_pk_bf16_f32 v184, v184, v185\n"
      "v_mfma_f32_16x16x32_bf16 v[236:239], a[40:43], v[72:75], v[236:239]\n"
      "ds_read_b128 v[68:71], v96 offset:64\n"
      "v_fma_f32 v190, v190, s55, v105\n"
      "v_mfma_f32_16x16x32_bf16 v[240:243], a[72:75], v[72:75], v[240:243]\n"
      "v_exp_f32 v186, v186\n"
      "v_mfma_f32_16x16x32_bf16 v[240:243], a[76:79], v[76:79], v[240:243]\n"
      "v_exp_f32 v187, v187\n"
      "v_mfma_f32_16x16x32_bf16 v[244:247], a[108:111], v[76:79], v[244:247]\n"
      "buffer_load_dwordx4 v112, s[44:47], s64 offen offset:1024 nt lds\n"
      "v_add_f32 v106, v106, v186\n"
      "v_mfma_f32_16x16x32_bf16 v[244:247], a[104:107], v[72:75], v[244:247]\n"
      "v_add_f32 v106, v106, v187\n"
      "v_cvt_pk_bf16_f32 v185, v186, v187\n"
      "v_mfma_f32_16x16x32_bf16 v[248:251], a[136:139], v[72:75], v[248:251]\n"
      "v_exp_f32 v188, v188\n"
      "v_mfma_f32_16x16x32_bf16 v[248:251], a[140:143], v[76:79], v[248:251]\n"
      "v_exp_f32 v189, v189\n"
      "v_mfma_f32_16x16x32_bf16 v[252:255], a[172:175], v[76:79], v[252:255]\n"
      "v_add_f32 v106, v106, v188\n"
      "v_fma_f32 v191, v191, s55, v105\n"
      "v_mfma_f32_16x16x32_bf16 v[252:255], a[168:171], v[72:75], v[252:255]\n"
      "buffer_load_dwordx4 v113, s[44:47], s64 offen offset:2048 nt lds\n"
      "v_add_f32 v106, v106, v189\n"
      "v_mfma_f32_16x16x32_bf16 v[232:235], a[16:19], v[80:83], v[232:235]\n"
      "v_cvt_pk_bf16_f32 v186, v188, v189\n"
      "v_fma_f32 v192, v192, s55, v105\n"
      "v_mfma_f32_16x16x32_bf16 v[232:235], a[20:23], v[84:87], v[232:235]\n"
      "ds_read_b128 v[72:75], v96 offset:128\n"
      "v_fma_f32 v193, v193, s55, v105\n"
      "v_mfma_f32_16x16x32_bf16 v[236:239], a[52:55], v[84:87], v[236:239]\n"
      "v_exp_f32 v190, v190\n"
      "v_mfma_f32_16x16x32_bf16 v[236:239], a[48:51], v[80:83], v[236:239]\n"
      "ds_read_b128 v[76:79], v96 offset:192\n"
      "v_fma_f32 v194, v194, s55, v105\n"
      "v_mfma_f32_16x16x32_bf16 v[240:243], a[80:83], v[80:83], v[240:243]\n"
      "buffer_load_dwordx4 v112, s[44:47], s64 offen offset:3072 nt lds\n"
      "v_add_f32 v106, v106, v190\n"
      "v_mfma_f32_16x16x32_bf16 v[240:243], a[84:87], v[84:87], v[240:243]\n"
      "v_exp_f32 v191, v191\n"
      "v_mfma_f32_16x16x32_bf16 v[244:247], a[116:119], v[84:87], v[244:247]\n"
      "v_exp_f32 v192, v192\n"
      "v_mfma_f32_16x16x32_bf16 v[244:247], a[112:115], v[80:83], v[244:247]\n"
      "v_add_f32 v106, v106, v191\n"
      "v_cvt_pk_bf16_f32 v187, v190, v191\n"
      "v_mfma_f32_16x16x32_bf16 v[248:251], a[144:147], v[80:83], v[248:251]\n"
      "v_add_f32 v106, v106, v192\n"
      "v_fma_f32 v195, v195, s55, v105\n"
      "v_mfma_f32_16x16x32_bf16 v[248:251], a[148:151], v[84:87], v[248:251]\n"
      "ds_write_b128 v125, v[184:187] offset:3072\n"
      "v_mfma_f32_16x16x32_bf16 v[252:255], a[180:183], v[84:87], v[252:255]\n"
      "v_exp_f32 v193, v193\n"
      "v_mfma_f32_16x16x32_bf16 v[252:255], a[176:179], v[80:83], v[252:255]\n"
      "v_exp_f32 v194, v194\n"
      "v_mfma_f32_16x16x32_bf16 v[232:235], a[24:27], v[88:91], v[232:235]\n"
      "s_waitcnt vmcnt(12)\n"
      "v_add_f32 v106, v106, v193\n"
      "v_cvt_pk_bf16_f32 v192, v192, v193\n"
      "v_mfma_f32_16x16x32_bf16 v[232:235], a[28:31], v[92:95], v[232:235]\n"
      "ds_read_b64_tr_b16 a[240:241], v114 offset:12672\n"
      "v_add_f32 v106, v106, v194\n"
      "v_mfma_f32_16x16x32_bf16 v[236:239], a[60:63], v[92:95], v[236:239]\n"
      "ds_read_b64_tr_b16 a[242:243], v114 offset:13184\n"
      "v_fma_f32 v196, v196, s55, v105\n"
      "v_mfma_f32_16x16x32_bf16 v[236:239], a[56:59], v[88:91], v[236:239]\n"
      "ds_read_b64_tr_b16 a[244:245], v114 offset:12680\n"
      "v_fma_f32 v197, v197, s55, v105\n"
      "v_mfma_f32_16x16x32_bf16 v[240:243], a[88:91], v[88:91], v[240:243]\n"
      "ds_read_b64_tr_b16 a[246:247], v114 offset:13192\n"
      "v_fma_f32 v198, v198, s55, v105\n"
      "v_mfma_f32_16x16x32_bf16 v[240:243], a[92:95], v[92:95], v[240:243]\n"
      "ds_read_b64_tr_b16 a[248:249], v115 offset:12672\n"
      "v_max3_f32 v108, v232, v233, v234\n"
      "v_mfma_f32_16x16x32_bf16 v[244:247], a[124:127], v[92:95], v[244:247]\n"
      "ds_read_b64_tr_b16 a[250:251], v115 offset:13184\n"
      "v_fma_f32 v199, v199, s55, v105\n"
      "v_mfma_f32_16x16x32_bf16 v[244:247], a[120:123], v[88:91], v[244:247]\n"
      "ds_read_b64_tr_b16 a[252:253], v115 offset:12680\n"
      "v_max3_f32 v108, v108, v235, v236\n"
      "v_mfma_f32_16x16x32_bf16 v[248:251], a[152:155], v[88:91], v[248:251]\n"
      "ds_read_b64_tr_b16 a[254:255], v115 offset:13192\n"
      "v_max3_f32 v108, v108, v237, v238\n"
      "v_mfma_f32_16x16x32_bf16 v[248:251], a[156:159], v[92:95], v[248:251]\n"
      "ds_read_b128 v[80:83], v96 offset:256\n"
      "v_max3_f32 v108, v108, v239, v240\n"
      "v_mfma_f32_16x16x32_bf16 v[252:255], a[188:191], v[92:95], v[252:255]\n"
      "ds_read_b128 v[84:87], v96 offset:320\n"
      "v_max3_f32 v108, v108, v241, v242\n"
      "v_mfma_f32_16x16x32_bf16 v[252:255], a[184:187], v[88:91], v[252:255]\n"
      "v_max3_f32 v108, v108, v243, v244\n"
      "v_max3_f32 v108, v108, v245, v246\n"
      "s_waitcnt lgkmcnt(14)\n"
      "v_mfma_f32_16x16x32_bf16 v[24:27], a[0:3], v[64:67], 0\n"
      "s_add_u32 m0, s53, 12672\n"
      "s_mov_b32 s64, 114688\n"
      "v_fma_f32 v200, v200, s55, v105\n"
      "s_waitcnt lgkmcnt(13)\n"
      "v_mfma_f32_16x16x32_bf16 v[24:27], a[4:7], v[68:71], v[24:27]\n"
      "s_waitcnt lgkmcnt(2)\n"
      "buffer_load_dwordx4 v113, s[44:47], s64 offen nt lds\n"
      "v_max3_f32 v108, v108, v247, v248\n"
      "v_mfma_f32_16x16x32_bf16 v[28:31], a[36:39], v[68:71], 0\n"
      "ds_read_b128 v[88:91], v96 offset:384\n"
      "v_max3_f32 v108, v108, v249, v250\n"
      "v_mfma_f32_16x16x32_bf16 v[28:31], a[32:35], v[64:67], v[28:31]\n"
      "ds_read_b128 v[92:95], v96 offset:448\n"
      "v_max3_f32 v108, v108, v251, v252\n"
      "v_mfma_f32_16x16x32_bf16 v[32:35], a[64:67], v[64:67], 0\n"
      "v_max3_f32 v108, v108, v253, v254\n"
      "v_max_f32 v108, v108, v255\n"
      "v_mfma_f32_16x16x32_bf16 v[32:35], a[68:71], v[68:71], v[32:35]\n"
      "ds_max_f32 v104, v108 offset:384\n"
      "v_fma_f32 v201, v201, s55, v105\n"
      "v_mfma_f32_16x16x32_bf16 v[36:39], a[100:103], v[68:71], 0\n"
      "buffer_load_dwordx4 v112, s[44:47], s64 offen offset:1024 nt lds\n"
      "v_fma_f32 v202, v202, s55, v105\n"
      "v_mfma_f32_16x16x32_bf16 v[36:39], a[96:99], v[64:67], v[36:39]\n"
      "v_exp_f32 v195, v195\n"
      "v_mfma_f32_16x16x32_bf16 v[40:43], a[128:131], v[64:67], 0\n"
      "v_exp_f32 v196, v196\n"
      "v_mfma_f32_16x16x32_bf16 v[40:43], a[132:135], v[68:71], v[40:43]\n"
      "v_add_f32 v106, v106, v195\n"
      "v_cvt_pk_bf16_f32 v193, v194, v195\n"
      "v_mfma_f32_16x16x32_bf16 v[44:47], a[164:167], v[68:71], 0\n"
      "v_add_f32 v106, v106, v196\n"
      "v_fma_f32 v203, v203, s55, v105\n"
      "v_mfma_f32_16x16x32_bf16 v[44:47], a[160:163], v[64:67], v[44:47]\n"
      "buffer_load_dwordx4 v113, s[44:47], s64 offen offset:2048 nt lds\n"
      "v_fma_f32 v204, v204, s55, v105\n"
      "v_mfma_f32_16x16x32_bf16 v[24:27], a[8:11], v[72:75], v[24:27]\n"
      "v_exp_f32 v197, v197\n"
      "v_mfma_f32_16x16x32_bf16 v[24:27], a[12:15], v[76:79], v[24:27]\n"
      "v_exp_f32 v198, v198\n"
      "v_mfma_f32_16x16x32_bf16 v[28:31], a[44:47], v[76:79], v[28:31]\n"
      "v_add_f32 v106, v106, v197\n"
      "v_cvt_pk_bf16_f32 v194, v196, v197\n"
      "v_mfma_f32_16x16x32_bf16 v[28:31], a[40:43], v[72:75], v[28:31]\n"
      "v_add_f32 v106, v106, v198\n"
      "v_fma_f32 v205, v205, s55, v105\n"
      "v_mfma_f32_16x16x32_bf16 v[32:35], a[72:75], v[72:75], v[32:35]\n"
      "buffer_load_dwordx4 v112, s[44:47], s64 offen offset:3072 nt lds\n"
      "v_fma_f32 v206, v206, s55, v105\n"
      "v_mfma_f32_16x16x32_bf16 v[32:35], a[76:79], v[76:79], v[32:35]\n"
      "v_exp_f32 v199, v199\n"
      "v_mfma_f32_16x16x32_bf16 v[36:39], a[108:111], v[76:79], v[36:39]\n"
      "v_exp_f32 v200, v200\n"
      "v_mfma_f32_16x16x32_bf16 v[36:39], a[104:107], v[72:75], v[36:39]\n"
      "v_add_f32 v106, v106, v199\n"
      "v_cvt_pk_bf16_f32 v195, v198, v199\n"
      "v_mfma_f32_16x16x32_bf16 v[40:43], a[136:139], v[72:75], v[40:43]\n"
      "v_add_f32 v106, v106, v200\n"
      "v_fma_f32 v207, v207, s55, v105\n"
      "v_mfma_f32_16x16x32_bf16 v[40:43], a[140:143], v[76:79], v[40:43]\n"
      "ds_write_b128 v125, v[192:195] offset:4096\n"
      "v_mfma_f32_16x16x32_bf16 v[44:47], a[172:175], v[76:79], v[44:47]\n"
      "v_fma_f32 v105, v119, s56, v111\n"
      "v_mfma_f32_16x16x32_bf16 v[44:47], a[168:171], v[72:75], v[44:47]\n"
      "v_exp_f32 v201, v201\n"
      "s_waitcnt lgkmcnt(5)\n"
      "v_mfma_f32_16x16x32_bf16 v[24:27], a[16:19], v[80:83], v[24:27]\n"
      "s_waitcnt vmcnt(12)\n"
      "v_exp_f32 v202, v202\n"
      "s_waitcnt lgkmcnt(4)\n"
      "v_mfma_f32_16x16x32_bf16 v[24:27], a[20:23], v[84:87], v[24:27]\n"
      "ds_read_b64_tr_b16 v[128:129], v114\n"
      "v_add_f32 v106, v106, v201\n"
      "v_mfma_f32_16x16x32_bf16 v[28:31], a[52:55], v[84:87], v[28:31]\n"
      "ds_read_b64_tr_b16 v[130:131], v114 offset:512\n"
      "v_cvt_pk_bf16_f32 v200, v200, v201\n"
      "v_mfma_f32_16x16x32_bf16 v[28:31], a[48:51], v[80:83], v[28:31]\n"
      "ds_read_b64_tr_b16 v[132:133], v114 offset:8\n"
      "v_add_f32 v106, v106, v202\n"
      "v_mfma_f32_16x16x32_bf16 v[32:35], a[80:83], v[80:83], v[32:35]\n"
      "ds_read_b64_tr_b16 v[134:135], v114 offset:520\n"
      "v_fma_f32 v208, v208, s55, v105\n"
      "v_mfma_f32_16x16x32_bf16 v[32:35], a[84:87], v[84:87], v[32:35]\n"
      "ds_read_b64_tr_b16 v[136:137], v115\n"
      "v_fma_f32 v209, v209, s55, v105\n"
      "v_mfma_f32_16x16x32_bf16 v[36:39], a[116:119], v[84:87], v[36:39]\n"
      "ds_read_b64_tr_b16 v[138:139], v115 offset:512\n"
      "v_fma_f32 v210, v210, s55, v105\n"
      "v_mfma_f32_16x16x32_bf16 v[36:39], a[112:115], v[80:83], v[36:39]\n"
      "ds_read_b64_tr_b16 v[140:141], v115 offset:8\n"
      "v_fma_f32 v211, v211, s55, v105\n"
      "v_mfma_f32_16x16x32_bf16 v[40:43], a[144:147], v[80:83], v[40:43]\n"
      "ds_read_b64_tr_b16 v[142:143], v115 offset:520\n"
      "v_fma_f32 v212, v212, s55, v105\n"
      "v_mfma_f32_16x16x32_bf16 v[40:43], a[148:151], v[84:87], v[40:43]\n"
      "v_exp_f32 v203, v203\n"
      "v_mfma_f32_16x16x32_bf16 v[44:47], a[180:183], v[84:87], v[44:47]\n"
      "v_exp_f32 v204, v204\n"
      "v_mfma_f32_16x16x32_bf16 v[44:47], a[176:179], v[80:83], v[44:47]\n"
      "v_add_f32 v106, v106, v203\n"
      "v_cvt_pk_bf16_f32 v201, v202, v203\n"
      "s_waitcnt lgkmcnt(11)\n"
      "v_mfma_f32_16x16x32_bf16 v[24:27], a[24:27], v[88:91], v[24:27]\n"
      "s_add_u32 m0, s53, 0\n"
      "s_mov_b32 s64, 131072\n"
      "v_add_f32 v106, v106, v204\n"
      "s_waitcnt lgkmcnt(10)\n"
      "v_mfma_f32_16x16x32_bf16 v[24:27], a[28:31], v[92:95], v[24:27]\n"
      "s_waitcnt lgkmcnt(0)\n"
      "buffer_load_dwordx4 v113, s[44:47], s64 offen nt lds\n"
      "v_fma_f32 v213, v213, s55, v105\n"
      "v_mfma_f32_16x16x32_bf16 v[28:31], a[60:63], v[92:95], v[28:31]\n"
      "v_exp_f32 v205, v205\n"
      "v_mfma_f32_16x16x32_bf16 v[28:31], a[56:59], v[88:91], v[28:31]\n"
      "v_exp_f32 v208, v208\n"
      "v_mfma_f32_16x16x32_bf16 v[32:35], a[88:91], v[88:91], v[32:35]\n"
      "v_add_f32 v106, v106, v205\n"
      "v_cvt_pk_bf16_f32 v202, v204, v205\n"
      "v_mfma_f32_16x16x32_bf16 v[32:35], a[92:95], v[92:95], v[32:35]\n"
      "v_max3_f32 v109, v24, v25, v26\n"
      "v_fma_f32 v214, v214, s55, v105\n"
      "v_mfma_f32_16x16x32_bf16 v[36:39], a[124:127], v[92:95], v[36:39]\n"
      "buffer_load_dwordx4 v112, s[44:47], s64 offen offset:1024 nt lds\n"
      "v_fma_f32 v215, v215, s55, v105\n"
      "v_mfma_f32_16x16x32_bf16 v[36:39], a[120:123], v[88:91], v[36:39]\n"
      "v_max3_f32 v109, v109, v27, v28\n"
      "v_max3_f32 v109, v109, v29, v30\n"
      "v_mfma_f32_16x16x32_bf16 v[40:43], a[152:155], v[88:91], v[40:43]\n"
      "v_exp_f32 v206, v206\n"
      "v_mfma_f32_16x16x32_bf16 v[40:43], a[156:159], v[92:95], v[40:43]\n"
      "v_max3_f32 v109, v109, v31, v32\n"
      "v_max3_f32 v109, v109, v33, v34\n"
      "v_mfma_f32_16x16x32_bf16 v[44:47], a[188:191], v[92:95], v[44:47]\n"
      "v_add_f32 v106, v106, v206\n"
      "v_fma_f32 v216, v216, s55, v105\n"
      "v_mfma_f32_16x16x32_bf16 v[44:47], a[184:187], v[88:91], v[44:47]\n"
      "buffer_load_dwordx4 v113, s[44:47], s64 offen offset:2048 nt lds\n"
      "v_max3_f32 v109, v109, v35, v36\n"
      "v_max3_f32 v109, v109, v37, v38\n"
      "v_fma_f32 v217, v217, s55, v105\n"
      "v_max3_f32 v109, v109, v39, v40\n"
      "v_max3_f32 v109, v109, v41, v42\n"
      "v_exp_f32 v209, v209\n"
      "s_nop 0\n"
      "v_max3_f32 v109, v109, v43, v44\n"
      "v_max3_f32 v109, v109, v45, v46\n"
      "buffer_load_dwordx4 v112, s[44:47], s64 offen offset:3072 nt lds\n"
      "v_max_f32 v109, v109, v47\n"
      "ds_max_f32 v104, v109 offset:448\n"
      "v_fma_f32 v218, v218, s55, v105\n"
      "v_exp_f32 v207, v207\n"
      "v_exp_f32 v210, v210\n"
      "v_add_f32 v106, v106, v207\n"
      "v_cvt_pk_bf16_f32 v203, v206, v207\n"
      "v_exp_f32 v211, v211\n"
      "ds_write_b128 v125, v[200:203] offset:5120\n"
      "v_cvt_u32_f32 v110, v106\n"
      "v_add_f32 v106, v208, v209\n"
      "s_waitcnt vmcnt(12)\n"
      "v_cvt_pk_bf16_f32 v208, v208, v209\n"
      "v_add_f32 v106, v106, v210\n"
      "ds_read_b64_tr_b16 v[144:145], v114 offset:4224\n"
      "v_add_f32 v106, v106, v211\n"
      "ds_read_b64_tr_b16 v[146:147], v114 offset:4736\n"
      "v_cvt_pk_bf16_f32 v209, v210, v211\n"
      "ds_read_b64_tr_b16 v[148:149], v114 offset:4232\n"
      "v_fma_f32 v219, v219, s55, v105\n"
      "ds_read_b64_tr_b16 v[150:151], v114 offset:4744\n"
      "v_fma_f32 v220, v220, s55, v105\n"
      "ds_read_b64_tr_b16 v[152:153], v115 offset:4224\n"
      "v_fma_f32 v221, v221, s55, v105\n"
      "ds_read_b64_tr_b16 v[154:155], v115 offset:4736\n"
      "v_fma_f32 v222, v222, s55, v105\n"
      "ds_read_b64_tr_b16 v[156:157], v115 offset:4232\n"
      "v_fma_f32 v223, v223, s55, v105\n"
      "ds_read_b64_tr_b16 v[158:159], v115 offset:4744\n"
      "v_fma_f32 v224, v224, s55, v105\n"
      "ds_add_u32 v104, v110 offset:1792\n"
      "v_fma_f32 v225, v225, s55, v105\n"
      "v_exp_f32 v212, v212\n"
      "v_exp_f32 v213, v213\n"
      "s_add_u32 m0, s53, 4224\n"
      "s_mov_b32 s64, 147456\n"
      "v_add_f32 v106, v106, v212\n"
      "s_waitcnt lgkmcnt(1)\n"
      "buffer_load_dwordx4 v113, s[44:47], s64 offen nt lds\n"
      "v_add_f32 v106, v106, v213\n"
      "v_cvt_pk_bf16_f32 v210, v212, v213\n"
      "v_fma_f32 v226, v226, s55, v105\n"
      "v_exp_f32 v214, v214\n"
      "v_exp_f32 v215, v215\n"
      "v_add_f32 v106, v106, v214\n"
      "v_fma_f32 v227, v227, s55, v105\n"
      "v_add_f32 v106, v106, v215\n"
      "v_cvt_pk_bf16_f32 v211, v214, v215\n"
      "v_exp_f32 v216, v216\n"
      "buffer_load_dwordx4 v112, s[44:47], s64 offen offset:1024 nt lds\n"
      "v_fma_f32 v228, v228, s55, v105\n"
      "ds_write_b128 v99, v[208:211]\n"
      "v_add_f32 v106, v106, v216\n"
      "v_fma_f32 v229, v229, s55, v105\n"
      "v_exp_f32 v217, v217\n"
      "v_exp_f32 v218, v218\n"
      "v_add_f32 v106, v106, v217\n"
      "v_cvt_pk_bf16_f32 v216, v216, v217\n"
      "v_add_f32 v106, v106, v218\n"
      "v_fma_f32 v230, v230, s55, v105\n"
      "buffer_load_dwordx4 v113, s[44:47], s64 offen offset:2048 nt lds\n"
      "v_fma_f32 v231, v231, s55, v105\n"
      "v_exp_f32 v219, v219\n"
      "v_exp_f32 v220, v220\n"
      "v_add_f32 v106, v106, v219\n"
      "v_cvt_pk_bf16_f32 v217, v218, v219\n"
      "v_add_f32 v106, v106, v220\n"
      "v_exp_f32 v221, v221\n"
      "v_exp_f32 v222, v222\n"
      "buffer_load_dwordx4 v112, s[44:47], s64 offen offset:3072 nt lds\n"
      "v_add_f32 v106, v106, v221\n"
      "v_cvt_pk_bf16_f32 v218, v220, v221\n"
      "v_add_f32 v106, v106, v222\n"
      "v_exp_f32 v223, v223\n"
      "v_exp_f32 v224, v224\n"
      "v_add_f32 v106, v106, v223\n"
      "v_cvt_pk_bf16_f32 v219, v222, v223\n"
      "v_add_f32 v106, v106, v224\n"
      "ds_write_b128 v99, v[216:219] offset:1024\n"
      "v_exp_f32 v225, v225\n"
      "v_exp_f32 v226, v226\n"
      "v_add_f32 v106, v106, v225\n"
      "v_cvt_pk_bf16_f32 v224, v224, v225\n"
      "v_add_f32 v106, v106, v226\n"
      "v_exp_f32 v227, v227\n"
      "v_exp_f32 v228, v228\n"
      "v_add_f32 v106, v106, v227\n"
      "v_cvt_pk_bf16_f32 v225, v226, v227\n"
      "v_add_f32 v106, v106, v228\n"
      "v_exp_f32 v229, v229\n"
      "v_exp_f32 v230, v230\n"
      "v_add_f32 v106, v106, v229\n"
      "v_cvt_pk_bf16_f32 v226, v228, v229\n"
      "v_add_f32 v106, v106, v230\n"
      "v_exp_f32 v231, v231\n"
      "s_nop 0\n"
      "v_add_f32 v106, v106, v231\n"
      "v_cvt_pk_bf16_f32 v227, v230, v231\n"
      "ds_write_b128 v99, v[224:227] offset:2048\n"
      "v_cvt_u32_f32 v110, v106\n"
      "ds_add_u32 v104, v110 offset:1856\n"
      "v_add_u32 v124, 147712, v98\n"
      "v_add_u32 v127, 50688, v98\n"
      "v_add_u32 v126, 105600, v98\n"
      "s_waitcnt lgkmcnt(0)\n"
      "s_barrier\n"
      "ds_read_b32 v125, v104 offset:384\n"
      "ds_read_b32 v107, v104 offset:1664\n"
      "ds_read_b32 v108, v104 offset:1728\n"
      "ds_read_b32 v109, v104 offset:1792\n"
      "ds_read_b32 v96, v104 offset:1856\n"
      "ds_read_b128 v[64:67], v124\n"
      "ds_read_b128 v[68:71], v124 offset:1024\n"
      "s_waitcnt lgkmcnt(1)\n"
      "v_mfma_f32_16x16x32_bf16 v[48:51], a[192:195], v[64:67], 0\n"
      "ds_read_b128 v[72:75], v127\n"
      "s_waitcnt vmcnt(12)\n"
      "s_waitcnt lgkmcnt(1)\n"
      "v_mfma_f32_16x16x32_bf16 v[48:51], a[208:211], v[68:71], v[48:51]\n"
      "ds_read_b128 v[76:79], v127 offset:1024\n"
      "ds_read_b64_tr_b16 v[160:161], v114 offset:8448\n"
      "v_mfma_f32_16x16x32_bf16 v[52:55], a[196:199], v[64:67], 0\n"
      "ds_read_b64_tr_b16 v[162:163], v114 offset:8960\n"
      "v_cvt_f32_u32 v107, v107\n"
      "v_mfma_f32_16x16x32_bf16 v[52:55], a[212:215], v[68:71], v[52:55]\n"
      "ds_read_b64_tr_b16 v[164:165], v114 offset:8456\n"
      "v_cvt_f32_u32 v108, v108\n"
      "v_mfma_f32_16x16x32_bf16 v[56:59], a[200:203], v[64:67], 0\n"
      "ds_read_b64_tr_b16 v[166:167], v114 offset:8968\n"
      "v_fma_f32 v105, v125, s56, v111\n"
      "v_mfma_f32_16x16x32_bf16 v[56:59], a[216:219], v[68:71], v[56:59]\n"
      "ds_read_b64_tr_b16 v[168:169], v115 offset:8448\n"
      "v_fma_f32 v232, v232, s55, v105\n"
      "v_mfma_f32_16x16x32_bf16 v[60:63], a[204:207], v[64:67], 0\n"
      "ds_read_b64_tr_b16 v[170:171], v115 offset:8960\n"
      "v_fma_f32 v233, v233, s55, v105\n"
      "v_mfma_f32_16x16x32_bf16 v[60:63], a[220:223], v[68:71], v[60:63]\n"
      "ds_read_b64_tr_b16 v[172:173], v115 offset:8456\n"
      "v_fma_f32 v234, v234, s55, v105\n"
      "s_waitcnt lgkmcnt(8)\n"
      "v_mfma_f32_16x16x32_bf16 v[80:83], a[192:195], v[72:75], 0\n"
      "ds_read_b128 v[64:67], v127 offset:3072\n"
      "ds_read_b64_tr_b16 v[174:175], v115 offset:8968\n"
      "s_waitcnt lgkmcnt(9)\n"
      "v_mfma_f32_16x16x32_bf16 v[80:83], a[208:211], v[76:79], v[80:83]\n"
      "ds_read_b128 v[68:71], v127 offset:4096\n"
      "v_fma_f32 v235, v235, s55, v105\n"
      "v_mfma_f32_16x16x32_bf16 v[84:87], a[196:199], v[72:75], 0\n"
      "v_exp_f32 v232, v232\n"
      "v_mfma_f32_16x16x32_bf16 v[84:87], a[212:215], v[76:79], v[84:87]\n"
      "v_exp_f32 v233, v233\n"
      "v_mfma_f32_16x16x32_bf16 v[88:91], a[200:203], v[72:75], 0\n"
      "s_add_u32 m0, s53, 8448\n"
      "s_mov_b32 s64, 163840\n"
      "v_fma_f32 v236, v236, s55, v105\n"
      "v_mfma_f32_16x16x32_bf16 v[88:91], a[216:219], v[76:79], v[88:91]\n"
      "s_waitcnt lgkmcnt(1)\n"
      "buffer_load_dwordx4 v113, s[44:47], s64 offen nt lds\n"
      "v_add_f32 v106, v232, v233\n"
      "v_mfma_f32_16x16x32_bf16 v[92:95], a[204:207], v[72:75], 0\n"
      "v_cvt_pk_bf16_f32 v232, v232, v233\n"
      "v_fma_f32 v237, v237, s55, v105\n"
      "v_mfma_f32_16x16x32_bf16 v[92:95], a[220:223], v[76:79], v[92:95]\n"
      "v_exp_f32 v234, v234\n"
      "v_mfma_f32_16x16x32_bf16 v[0:3], a[192:195], v[64:67], 0\n"
      "ds_read_b128 v[72:75], v98\n"
      "v_fma_f32 v238, v238, s55, v105\n"
      "s_waitcnt lgkmcnt(1)\n"
      "v_mfma_f32_16x16x32_bf16 v[0:3], a[208:211], v[68:71], v[0:3]\n"
      "ds_read_b128 v[76:79], v98 offset:1024\n"
      "buffer_load_dwordx4 v112, s[44:47], s64 offen offset:1024 nt lds\n"
      "v_mfma_f32_16x16x32_bf16 v[4:7], a[196:199], v[64:67], 0\n"
      "v_add_f32 v106, v106, v234\n"
      "v_fma_f32 v239, v239, s55, v105\n"
      "v_mfma_f32_16x16x32_bf16 v[4:7], a[212:215], v[68:71], v[4:7]\n"
      "v_exp_f32 v235, v235\n"
      "v_mfma_f32_16x16x32_bf16 v[8:11], a[200:203], v[64:67], 0\n"
      "v_exp_f32 v236, v236\n"
      "v_mfma_f32_16x16x32_bf16 v[8:11], a[216:219], v[68:71], v[8:11]\n"
      "buffer_load_dwordx4 v113, s[44:47], s64 offen offset:2048 nt lds\n"
      "v_add_f32 v106, v106, v235\n"
      "v_mfma_f32_16x16x32_bf16 v[12:15], a[204:207], v[64:67], 0\n"
      "v_cvt_pk_bf16_f32 v233, v234, v235\n"
      "v_add_f32 v106, v106, v236\n"
      "v_mfma_f32_16x16x32_bf16 v[12:15], a[220:223], v[68:71], v[12:15]\n"
      "v_exp_f32 v237, v237\n"
      "s_waitcnt lgkmcnt(1)\n"
      "v_mfma_f32_16x16x32_bf16 v[16:19], a[192:195], v[72:75], 0\n"
      "ds_read_b128 v[64:67], v124 offset:2048\n"
      "v_fma_f32 v240, v240, s55, v105\n"
      "s_waitcnt lgkmcnt(1)\n"
      "v_mfma_f32_16x16x32_bf16 v[16:19], a[208:211], v[76:79], v[16:19]\n"
      "ds_read_b128 v[68:71], v124 offset:3072\n"
      "buffer_load_dwordx4 v112, s[44:47], s64 offen offset:3072 nt lds\n"
      "v_mfma_f32_16x16x32_bf16 v[20:23], a[196:199], v[72:75], 0\n"
      "v_add_f32 v106, v106, v237\n"
      "v_cvt_pk_bf16_f32 v234, v236, v237\n"
      "v_mfma_f32_16x16x32_bf16 v[20:23], a[212:215], v[76:79], v[20:23]\n"
      "v_exp_f32 v238, v238\n"
      "v_mfma_f32_16x16x32_bf16 v[116:119], a[200:203], v[72:75], 0\n"
      "v_exp_f32 v239, v239\n"
      "v_mfma_f32_16x16x32_bf16 v[116:119], a[216:219], v[76:79], v[116:119]\n"
      "v_add_f32 v106, v106, v238\n"
      "v_fma_f32 v241, v241, s55, v105\n"
      "v_mfma_f32_16x16x32_bf16 v[120:123], a[204:207], v[72:75], 0\n"
      "v_add_f32 v106, v106, v239\n"
      "v_cvt_pk_bf16_f32 v235, v238, v239\n"
      "v_mfma_f32_16x16x32_bf16 v[120:123], a[220:223], v[76:79], v[120:123]\n"
      "v_exp_f32 v240, v240\n"
      "s_waitcnt lgkmcnt(1)\n"
      "v_mfma_f32_16x16x32_bf16 v[48:51], a[224:227], v[64:67], v[48:51]\n"
      "ds_read_b128 v[72:75], v127 offset:2048\n"
      "s_waitcnt vmcnt(12)\n"
      "s_waitcnt lgkmcnt(1)\n"
      "v_mfma_f32_16x16x32_bf16 v[48:51], a[240:243], v[68:71], v[48:51]\n"
      "ds_read_b128 v[76:79], v127 offset:27456\n"
      "ds_read_b64_tr_b16 v[176:177], v114 offset:12672\n"
      "v_mfma_f32_16x16x32_bf16 v[52:55], a[228:231], v[64:67], v[52:55]\n"
      "ds_read_b64_tr_b16 v[178:179], v114 offset:13184\n"
      "v_add_f32 v106, v106, v240\n"
      "v_mfma_f32_16x16x32_bf16 v[52:55], a[244:247], v[68:71], v[52:55]\n"
      "ds_read_b64_tr_b16 v[180:181], v114 offset:12680\n"
      "v_fma_f32 v242, v242, s55, v105\n"
      "v_mfma_f32_16x16x32_bf16 v[56:59], a[232:235], v[64:67], v[56:59]\n"
      "ds_read_b64_tr_b16 v[182:183], v114 offset:13192\n"
      "v_fma_f32 v243, v243, s55, v105\n"
      "v_mfma_f32_16x16x32_bf16 v[56:59], a[248:251], v[68:71], v[56:59]\n"
      "ds_read_b64_tr_b16 v[184:185], v115 offset:12672\n"
      "v_fma_f32 v244, v244, s55, v105\n"
      "v_mfma_f32_16x16x32_bf16 v[60:63], a[236:239], v[64:67], v[60:63]\n"
      "ds_read_b64_tr_b16 v[186:187], v115 offset:13184\n"
      "v_fma_f32 v245, v245, s55, v105\n"
      "v_mfma_f32_16x16x32_bf16 v[60:63], a[252:255], v[68:71], v[60:63]\n"
      "ds_read_b64_tr_b16 v[188:189], v115 offset:12680\n"
      "v_fma_f32 v246, v246, s55, v105\n"
      "s_waitcnt lgkmcnt(8)\n"
      "v_mfma_f32_16x16x32_bf16 v[80:83], a[224:227], v[72:75], v[80:83]\n"
      "ds_read_b128 v[64:67], v127 offset:5120\n"
      "ds_read_b64_tr_b16 v[190:191], v115 offset:13192\n"
      "s_waitcnt lgkmcnt(9)\n"
      "v_mfma_f32_16x16x32_bf16 v[80:83], a[240:243], v[76:79], v[80:83]\n"
      "ds_read_b128 v[68:71], v127 offset:30528\n"
      "v_fma_f32 v247, v247, s55, v105\n"
      "v_mfma_f32_16x16x32_bf16 v[84:87], a[228:231], v[72:75], v[84:87]\n"
      "ds_write_b128 v99, v[232:235] offset:3072\n"
      "v_mfma_f32_16x16x32_bf16 v[84:87], a[244:247], v[76:79], v[84:87]\n"
      "v_exp_f32 v241, v241\n"
      "v_mfma_f32_16x16x32_bf16 v[88:91], a[232:235], v[72:75], v[88:91]\n"
      "s_add_u32 m0, s53, 12672\n"
      "s_mov_b32 s64, 180224\n"
      "v_fma_f32 v248, v248, s55, v105\n"
      "v_mfma_f32_16x16x32_bf16 v[88:91], a[248:251], v[76:79], v[88:91]\n"
      "s_waitcnt lgkmcnt(2)\n"
      "buffer_load_dwordx4 v113, s[44:47], s64 offen nt lds\n"
      "v_add_f32 v106, v106, v241\n"
      "v_mfma_f32_16x16x32_bf16 v[92:95], a[236:239], v[72:75], v[92:95]\n"
      "v_cvt_pk_bf16_f32 v240, v240, v241\n"
      "v_fma_f32 v249, v249, s55, v105\n"
      "v_mfma_f32_16x16x32_bf16 v[92:95], a[252:255], v[76:79], v[92:95]\n"
      "v_exp_f32 v242, v242\n"
      "v_mfma_f32_16x16x32_bf16 v[0:3], a[224:227], v[64:67], v[0:3]\n"
      "ds_read_b128 v[72:75], v98 offset:2048\n"
      "v_fma_f32 v250, v250, s55, v105\n"
      "s_waitcnt lgkmcnt(2)\n"
      "v_mfma_f32_16x16x32_bf16 v[0:3], a[240:243], v[68:71], v[0:3]\n"
      "ds_read_b128 v[76:79], v98 offset:8448\n"
      "buffer_load_dwordx4 v112, s[44:47], s64 offen offset:1024 nt lds\n"
      "v_mfma_f32_16x16x32_bf16 v[4:7], a[228:231], v[64:67], v[4:7]\n"
      "v_add_f32 v106, v106, v242\n"
      "v_fma_f32 v251, v251, s55, v105\n"
      "v_mfma_f32_16x16x32_bf16 v[4:7], a[244:247], v[68:71], v[4:7]\n"
      "v_exp_f32 v243, v243\n"
      "v_mfma_f32_16x16x32_bf16 v[8:11], a[232:235], v[64:67], v[8:11]\n"
      "v_exp_f32 v244, v244\n"
      "v_mfma_f32_16x16x32_bf16 v[8:11], a[248:251], v[68:71], v[8:11]\n"
      "buffer_load_dwordx4 v113, s[44:47], s64 offen offset:2048 nt lds\n"
      "v_add_f32 v106, v106, v243\n"
      "v_mfma_f32_16x16x32_bf16 v[12:15], a[236:239], v[64:67], v[12:15]\n"
      "v_cvt_pk_bf16_f32 v241, v242, v243\n"
      "v_add_f32 v106, v106, v244\n"
      "v_mfma_f32_16x16x32_bf16 v[12:15], a[252:255], v[68:71], v[12:15]\n"
      "v_exp_f32 v245, v245\n"
      "s_waitcnt lgkmcnt(1)\n"
      "v_mfma_f32_16x16x32_bf16 v[16:19], a[224:227], v[72:75], v[16:19]\n"
      "ds_read_b128 v[64:67], v124 offset:4096\n"
      "v_fma_f32 v252, v252, s55, v105\n"
      "s_waitcnt lgkmcnt(1)\n"
      "v_mfma_f32_16x16x32_bf16 v[16:19], a[240:243], v[76:79], v[16:19]\n"
      "ds_read_b128 v[68:71], v124 offset:5120\n"
      "buffer_load_dwordx4 v112, s[44:47], s64 offen offset:3072 nt lds\n"
      "v_mfma_f32_16x16x32_bf16 v[20:23], a[228:231], v[72:75], v[20:23]\n"
      "v_add_f32 v106, v106, v245\n"
      "v_cvt_pk_bf16_f32 v242, v244, v245\n"
      "v_mfma_f32_16x16x32_bf16 v[20:23], a[244:247], v[76:79], v[20:23]\n"
      "v_exp_f32 v246, v246\n"
      "v_mfma_f32_16x16x32_bf16 v[116:119], a[232:235], v[72:75], v[116:119]\n"
      "v_exp_f32 v247, v247\n"
      "v_mfma_f32_16x16x32_bf16 v[116:119], a[248:251], v[76:79], v[116:119]\n"
      "v_add_f32 v106, v106, v246\n"
      "v_fma_f32 v253, v253, s55, v105\n"
      "v_mfma_f32_16x16x32_bf16 v[120:123], a[236:239], v[72:75], v[120:123]\n"
      "v_add_f32 v106, v106, v247\n"
      "v_cvt_pk_bf16_f32 v243, v246, v247\n"
      "v_mfma_f32_16x16x32_bf16 v[120:123], a[252:255], v[76:79], v[120:123]\n"
      "v_exp_f32 v248, v248\n"
      "s_waitcnt lgkmcnt(1)\n"
      "v_mfma_f32_16x16x32_bf16 v[48:51], v[128:131], v[64:67], v[48:51]\n"
      "ds_read_b128 v[72:75], v127 offset:28480\n"
      "s_waitcnt vmcnt(12)\n"
      "s_waitcnt lgkmcnt(1)\n"
      "v_mfma_f32_16x16x32_bf16 v[48:51], v[144:147], v[68:71], v[48:51]\n"
      "ds_read_b128 v[76:79], v127 offset:29504\n"
      "ds_read_b64_tr_b16 v[192:193], v114\n"
      "v_mfma_f32_16x16x32_bf16 v[52:55], v[132:135], v[64:67], v[52:55]\n"
      "ds_read_b64_tr_b16 v[194:195], v114 offset:512\n"
      "v_add_f32 v106, v106, v248\n"
      "v_mfma_f32_16x16x32_bf16 v[52:55], v[148:151], v[68:71], v[52:55]\n"
      "ds_read_b64_tr_b16 v[196:197], v114 offset:8\n"
      "v_fma_f32 v254, v254, s55, v105\n"
      "v_mfma_f32_16x16x32_bf16 v[56:59], v[136:139], v[64:67], v[56:59]\n"
      "ds_read_b64_tr_b16 v[198:199], v114 offset:520\n"
      "v_fma_f32 v255, v255, s55, v105\n"
      "v_mfma_f32_16x16x32_bf16 v[56:59], v[152:155], v[68:71], v[56:59]\n"
      "ds_read_b64_tr_b16 v[200:201], v115\n"
      "v_cvt_f32_u32 v109, v109\n"
      "v_mfma_f32_16x16x32_bf16 v[60:63], v[140:143], v[64:67], v[60:63]\n"
      "ds_read_b64_tr_b16 v[202:203], v115 offset:512\n"
      "v_cvt_f32_u32 v96, v96\n"
      "v_mfma_f32_16x16x32_bf16 v[60:63], v[156:159], v[68:71], v[60:63]\n"
      "ds_read_b64_tr_b16 v[204:205], v115 offset:8\n"
      "s_waitcnt lgkmcnt(8)\n"
      "v_mfma_f32_16x16x32_bf16 v[80:83], v[128:131], v[72:75], v[80:83]\n"
      "ds_read_b128 v[64:67], v127 offset:31552\n"
      "ds_read_b64_tr_b16 v[206:207], v115 offset:520\n"
      "s_waitcnt lgkmcnt(9)\n"
      "v_mfma_f32_16x16x32_bf16 v[80:83], v[144:147], v[76:79], v[80:83]\n"
      "ds_read_b128 v[68:71], v127 offset:32576\n"
      "v_mfma_f32_16x16x32_bf16 v[84:87], v[132:135], v[72:75], v[84:87]\n"
      "ds_write_b128 v99, v[240:243] offset:4096\n"
      "v_mfma_f32_16x16x32_bf16 v[84:87], v[148:151], v[76:79], v[84:87]\n"
      "v_exp_f32 v249, v249\n"
      "v_mfma_f32_16x16x32_bf16 v[88:91], v[136:139], v[72:75], v[88:91]\n"
      "s_add_u32 m0, s53, 23232\n"
      "v_exp_f32 v250, v250\n"
      "v_mfma_f32_16x16x32_bf16 v[88:91], v[152:155], v[76:79], v[88:91]\n"
      "buffer_load_dwordx4 v102, s[36:39], s57 offen nt lds\n"
      "v_add_f32 v106, v106, v249\n"
      "v_mfma_f32_16x16x32_bf16 v[92:95], v[140:143], v[72:75], v[92:95]\n"
      "v_cvt_pk_bf16_f32 v248, v248, v249\n"
      "v_add_f32 v106, v106, v250\n"
      "v_mfma_f32_16x16x32_bf16 v[92:95], v[156:159], v[76:79], v[92:95]\n"
      "v_exp_f32 v251, v251\n"
      "s_waitcnt lgkmcnt(3)\n"
      "v_mfma_f32_16x16x32_bf16 v[0:3], v[128:131], v[64:67], v[0:3]\n"
      "ds_read_b128 v[72:75], v98 offset:9472\n"
      "s_add_u32 m0, m0, 544\n"
      "s_waitcnt lgkmcnt(2)\n"
      "v_mfma_f32_16x16x32_bf16 v[0:3], v[144:147], v[68:71], v[0:3]\n"
      "ds_read_b128 v[76:79], v98 offset:10496\n"
      "buffer_load_dwordx4 v102, s[36:39], s57 offen offset:512 nt lds\n"
      "v_mfma_f32_16x16x32_bf16 v[4:7], v[132:135], v[64:67], v[4:7]\n"
      "v_add_f32 v106, v106, v251\n"
      "v_cvt_pk_bf16_f32 v249, v250, v251\n"
      "v_mfma_f32_16x16x32_bf16 v[4:7], v[148:151], v[68:71], v[4:7]\n"
      "v_exp_f32 v252, v252\n"
      "v_mfma_f32_16x16x32_bf16 v[8:11], v[136:139], v[64:67], v[8:11]\n"
      "s_add_u32 s57, s57, 8192\n"
      "s_add_u32 m0, s53, 25344\n"
      "v_mfma_f32_16x16x32_bf16 v[8:11], v[152:155], v[68:71], v[8:11]\n"
      "buffer_load_dwordx4 v102, s[36:39], s57 offen nt lds\n"
      "v_add_f32 v106, v106, v252\n"
      "v_mfma_f32_16x16x32_bf16 v[12:15], v[140:143], v[64:67], v[12:15]\n"
      "v_exp_f32 v253, v253\n"
      "v_mfma_f32_16x16x32_bf16 v[12:15], v[156:159], v[68:71], v[12:15]\n"
      "v_exp_f32 v254, v254\n"
      "s_waitcnt lgkmcnt(1)\n"
      "v_mfma_f32_16x16x32_bf16 v[16:19], v[128:131], v[72:75], v[16:19]\n"
      "ds_read_b128 v[64:67], v124 offset:6144\n"
      "s_add_u32 m0, m0, 544\n"
      "s_waitcnt lgkmcnt(1)\n"
      "v_mfma_f32_16x16x32_bf16 v[16:19], v[144:147], v[76:79], v[16:19]\n"
      "ds_read_b128 v[68:71], v124 offset:7168\n"
      "buffer_load_dwordx4 v102, s[36:39], s57 offen offset:512 nt lds\n"
      "v_mfma_f32_16x16x32_bf16 v[20:23], v[132:135], v[72:75], v[20:23]\n"
      "v_add_f32 v106, v106, v253\n"
      "v_cvt_pk_bf16_f32 v250, v252, v253\n"
      "v_mfma_f32_16x16x32_bf16 v[20:23], v[148:151], v[76:79], v[20:23]\n"
      "v_add_f32 v106, v106, v254\n"
      "v_mfma_f32_16x16x32_bf16 v[116:119], v[136:139], v[72:75], v[116:119]\n"
      "v_exp_f32 v255, v255\n"
      "v_mfma_f32_16x16x32_bf16 v[116:119], v[152:155], v[76:79], v[116:119]\n"
      "s_add_u32 s57, s57, 8192\n"
      "v_rcp_f32 v107, v107\n"
      "v_mfma_f32_16x16x32_bf16 v[120:123], v[140:143], v[72:75], v[120:123]\n"
      "v_add_f32 v106, v106, v255\n"
      "v_cvt_pk_bf16_f32 v251, v254, v255\n"
      "v_mfma_f32_16x16x32_bf16 v[120:123], v[156:159], v[76:79], v[120:123]\n"
      "v_rcp_f32 v108, v108\n"
      "s_waitcnt lgkmcnt(1)\n"
      "v_mfma_f32_16x16x32_bf16 v[48:51], v[160:163], v[64:67], v[48:51]\n"
      "ds_read_b128 v[72:75], v126\n"
      "s_waitcnt vmcnt(12)\n"
      "s_waitcnt lgkmcnt(1)\n"
      "v_mfma_f32_16x16x32_bf16 v[48:51], v[176:179], v[68:71], v[48:51]\n"
      "ds_read_b128 v[76:79], v126 offset:1024\n"
      "ds_read_b64_tr_b16 v[208:209], v114 offset:4224\n"
      "v_mfma_f32_16x16x32_bf16 v[52:55], v[164:167], v[64:67], v[52:55]\n"
      "ds_read_b64_tr_b16 v[210:211], v114 offset:4736\n"
      "v_cvt_u32_f32 v110, v106\n"
      "v_mfma_f32_16x16x32_bf16 v[52:55], v[180:183], v[68:71], v[52:55]\n"
      "ds_read_b64_tr_b16 v[212:213], v114 offset:4232\n"
      "v_mfma_f32_16x16x32_bf16 v[56:59], v[168:171], v[64:67], v[56:59]\n"
      "ds_read_b64_tr_b16 v[214:215], v114 offset:4744\n"
      "v_mfma_f32_16x16x32_bf16 v[56:59], v[184:187], v[68:71], v[56:59]\n"
      "ds_read_b64_tr_b16 v[216:217], v115 offset:4224\n"
      "v_mfma_f32_16x16x32_bf16 v[60:63], v[172:175], v[64:67], v[60:63]\n"
      "ds_read_b64_tr_b16 v[218:219], v115 offset:4736\n"
      "v_mfma_f32_16x16x32_bf16 v[60:63], v[188:191], v[68:71], v[60:63]\n"
      "ds_read_b64_tr_b16 v[220:221], v115 offset:4232\n"
      "s_waitcnt lgkmcnt(8)\n"
      "v_mfma_f32_16x16x32_bf16 v[80:83], v[160:163], v[72:75], v[80:83]\n"
      "ds_read_b128 v[64:67], v126 offset:3072\n"
      "ds_read_b64_tr_b16 v[222:223], v115 offset:4744\n"
      "s_waitcnt lgkmcnt(9)\n"
      "v_mfma_f32_16x16x32_bf16 v[80:83], v[176:179], v[76:79], v[80:83]\n"
      "ds_read_b128 v[68:71], v126 offset:4096\n"
      "v_mfma_f32_16x16x32_bf16 v[84:87], v[164:167], v[72:75], v[84:87]\n"
      "ds_write_b128 v99, v[248:251] offset:5120\n"
      "v_mfma_f32_16x16x32_bf16 v[84:87], v[180:183], v[76:79], v[84:87]\n"
      "ds_add_u32 v104, v110 offset:1920\n"
      "v_mfma_f32_16x16x32_bf16 v[88:91], v[168:171], v[72:75], v[88:91]\n"
      "v_rcp_f32 v109, v109\n"
      "v_mfma_f32_16x16x32_bf16 v[88:91], v[184:187], v[76:79], v[88:91]\n"
      "v_rcp_f32 v96, v96\n"
      "v_mfma_f32_16x16x32_bf16 v[92:95], v[172:175], v[72:75], v[92:95]\n"
      "v_mfma_f32_16x16x32_bf16 v[92:95], v[188:191], v[76:79], v[92:95]\n"
      "s_waitcnt lgkmcnt(4)\n"
      "v_mfma_f32_16x16x32_bf16 v[0:3], v[160:163], v[64:67], v[0:3]\n"
      "ds_read_b128 v[72:75], v98 offset:16896\n"
      "s_waitcnt lgkmcnt(3)\n"
      "v_mfma_f32_16x16x32_bf16 v[0:3], v[176:179], v[68:71], v[0:3]\n"
      "ds_read_b128 v[76:79], v98 offset:17920\n"
      "v_mfma_f32_16x16x32_bf16 v[4:7], v[164:167], v[64:67], v[4:7]\n"
      "v_mfma_f32_16x16x32_bf16 v[4:7], v[180:183], v[68:71], v[4:7]\n"
      "v_mfma_f32_16x16x32_bf16 v[8:11], v[168:171], v[64:67], v[8:11]\n"
      "v_mfma_f32_16x16x32_bf16 v[8:11], v[184:187], v[68:71], v[8:11]\n"
      "v_mfma_f32_16x16x32_bf16 v[12:15], v[172:175], v[64:67], v[12:15]\n"
      "v_mfma_f32_16x16x32_bf16 v[12:15], v[188:191], v[68:71], v[12:15]\n"
      "s_waitcnt lgkmcnt(1)\n"
      "v_mfma_f32_16x16x32_bf16 v[16:19], v[160:163], v[72:75], v[16:19]\n"
      "ds_read_b128 v[64:67], v124 offset:8192\n"
      "s_waitcnt lgkmcnt(1)\n"
      "v_mfma_f32_16x16x32_bf16 v[16:19], v[176:179], v[76:79], v[16:19]\n"
      "ds_read_b128 v[68:71], v124 offset:9216\n"
      "v_mfma_f32_16x16x32_bf16 v[20:23], v[164:167], v[72:75], v[20:23]\n"
      "v_mfma_f32_16x16x32_bf16 v[20:23], v[180:183], v[76:79], v[20:23]\n"
      "v_mfma_f32_16x16x32_bf16 v[116:119], v[168:171], v[72:75], v[116:119]\n"
      "v_mfma_f32_16x16x32_bf16 v[116:119], v[184:187], v[76:79], v[116:119]\n"
      "v_mfma_f32_16x16x32_bf16 v[120:123], v[172:175], v[72:75], v[120:123]\n"
      "v_mfma_f32_16x16x32_bf16 v[120:123], v[188:191], v[76:79], v[120:123]\n"
      "s_waitcnt lgkmcnt(1)\n"
      "v_mfma_f32_16x16x32_bf16 v[48:51], v[192:195], v[64:67], v[48:51]\n"
      "ds_read_b128 v[72:75], v126 offset:2048\n"
      "s_waitcnt lgkmcnt(1)\n"
      "v_mfma_f32_16x16x32_bf16 v[48:51], v[208:211], v[68:71], v[48:51]\n"
      "ds_read_b128 v[76:79], v126 offset:27456\n"
      "v_mfma_f32_16x16x32_bf16 v[52:55], v[196:199], v[64:67], v[52:55]\n"
      "s_waitcnt vmcnt(8)\n"
      "v_mfma_f32_16x16x32_bf16 v[52:55], v[212:215], v[68:71], v[52:55]\n"
      "ds_read_b64_tr_b16 v[224:225], v114 offset:8448\n"
      "v_mfma_f32_16x16x32_bf16 v[56:59], v[200:203], v[64:67], v[56:59]\n"
      "ds_read_b64_tr_b16 v[226:227], v114 offset:8960\n"
      "v_mfma_f32_16x16x32_bf16 v[56:59], v[216:219], v[68:71], v[56:59]\n"
      "ds_read_b64_tr_b16 v[228:229], v114 offset:8456\n"
      "v_mfma_f32_16x16x32_bf16 v[60:63], v[204:207], v[64:67], v[60:63]\n"
      "ds_read_b64_tr_b16 v[230:231], v114 offset:8968\n"
      "v_mfma_f32_16x16x32_bf16 v[60:63], v[220:223], v[68:71], v[60:63]\n"
      "ds_read_b64_tr_b16 v[232:233], v115 offset:8448\n"
      "s_waitcnt lgkmcnt(6)\n"
      "v_mfma_f32_16x16x32_bf16 v[80:83], v[192:195], v[72:75], v[80:83]\n"
      "ds_read_b128 v[64:67], v126 offset:5120\n"
      "ds_read_b64_tr_b16 v[234:235], v115 offset:8960\n"
      "s_waitcnt lgkmcnt(7)\n"
      "v_mfma_f32_16x16x32_bf16 v[80:83], v[208:211], v[76:79], v[80:83]\n"
      "ds_read_b128 v[68:71], v126 offset:30528\n"
      "ds_read_b64_tr_b16 v[236:237], v115 offset:8456\n"
      "v_mfma_f32_16x16x32_bf16 v[84:87], v[196:199], v[72:75], v[84:87]\n"
      "ds_read_b64_tr_b16 v[238:239], v115 offset:8968\n"
      "v_mfma_f32_16x16x32_bf16 v[84:87], v[212:215], v[76:79], v[84:87]\n"
      "v_mfma_f32_16x16x32_bf16 v[88:91], v[200:203], v[72:75], v[88:91]\n"
      "v_mfma_f32_16x16x32_bf16 v[88:91], v[216:219], v[76:79], v[88:91]\n"
      "v_mfma_f32_16x16x32_bf16 v[92:95], v[204:207], v[72:75], v[92:95]\n"
      "s_add_u32 m0, s53, 0\n"
      "v_mfma_f32_16x16x32_bf16 v[92:95], v[220:223], v[76:79], v[92:95]\n"
      "buffer_load_dwordx4 v102, s[36:39], s57 offen nt lds\n"
      "s_waitcnt lgkmcnt(4)\n"
      "v_mfma_f32_16x16x32_bf16 v[0:3], v[192:195], v[64:67], v[0:3]\n"
      "ds_read_b128 v[72:75], v98 offset:18944\n"
      "s_waitcnt lgkmcnt(3)\n"
      "v_mfma_f32_16x16x32_bf16 v[0:3], v[208:211], v[68:71], v[0:3]\n"
      "ds_read_b128 v[76:79], v98 offset:25344\n"
      "s_add_u32 m0, m0, 544\n"
      "v_mfma_f32_16x16x32_bf16 v[4:7], v[196:199], v[64:67], v[4:7]\n"
      "buffer_load_dwordx4 v102, s[36:39], s57 offen offset:512 nt lds\n"
      "v_mfma_f32_16x16x32_bf16 v[4:7], v[212:215], v[68:71], v[4:7]\n"
      "v_mfma_f32_16x16x32_bf16 v[8:11], v[200:203], v[64:67], v[8:11]\n"
      "v_mfma_f32_16x16x32_bf16 v[8:11], v[216:219], v[68:71], v[8:11]\n"
      "s_add_u32 s57, s57, 8192\n"
      "s_add_u32 m0, s53, 2112\n"
      "v_mfma_f32_16x16x32_bf16 v[12:15], v[204:207], v[64:67], v[12:15]\n"
      "buffer_load_dwordx4 v102, s[36:39], s57 offen nt lds\n"
      "v_mfma_f32_16x16x32_bf16 v[12:15], v[220:223], v[68:71], v[12:15]\n"
      "s_waitcnt lgkmcnt(1)\n"
      "v_mfma_f32_16x16x32_bf16 v[16:19], v[192:195], v[72:75], v[16:19]\n"
      "s_waitcnt lgkmcnt(0)\n"
      "v_mfma_f32_16x16x32_bf16 v[16:19], v[208:211], v[76:79], v[16:19]\n"
      "s_add_u32 m0, m0, 544\n"
      "v_mfma_f32_16x16x32_bf16 v[20:23], v[196:199], v[72:75], v[20:23]\n"
      "buffer_load_dwordx4 v102, s[36:39], s57 offen offset:512 nt lds\n"
      "v_mfma_f32_16x16x32_bf16 v[20:23], v[212:215], v[76:79], v[20:23]\n"
      "v_mfma_f32_16x16x32_bf16 v[116:119], v[200:203], v[72:75], v[116:119]\n"
      "v_mfma_f32_16x16x32_bf16 v[116:119], v[216:219], v[76:79], v[116:119]\n"
      "s_add_u32 s57, s57, 8192\n"
      "v_mfma_f32_16x16x32_bf16 v[120:123], v[204:207], v[72:75], v[120:123]\n"
      "v_mfma_f32_16x16x32_bf16 v[120:123], v[220:223], v[76:79], v[120:123]\n"
      "ds_read_b128 v[64:67], v126 offset:28480\n"
      "ds_read_b128 v[68:71], v126 offset:31552\n"
      "ds_read_b128 v[72:75], v98 offset:26368\n"
      "ds_read_b128 v[76:79], v124 offset:10240\n"
      "s_waitcnt lgkmcnt(3)\n"
      "v_mfma_f32_16x16x32_bf16 v[80:83], v[224:227], v[64:67], v[80:83]\n"
      "v_mfma_f32_16x16x32_bf16 v[84:87], v[228:231], v[64:67], v[84:87]\n"
      "v_mfma_f32_16x16x32_bf16 v[88:91], v[232:235], v[64:67], v[88:91]\n"
      "v_mfma_f32_16x16x32_bf16 v[92:95], v[236:239], v[64:67], v[92:95]\n"
      "s_waitcnt lgkmcnt(2)\n"
      "v_mfma_f32_16x16x32_bf16 v[0:3], v[224:227], v[68:71], v[0:3]\n"
      "ds_read_b128 v[64:67], v126 offset:29504\n"
      "v_mfma_f32_16x16x32_bf16 v[4:7], v[228:231], v[68:71], v[4:7]\n"
      "v_mfma_f32_16x16x32_bf16 v[8:11], v[232:235], v[68:71], v[8:11]\n"
      "s_waitcnt vmcnt(8)\n"
      "v_mfma_f32_16x16x32_bf16 v[12:15], v[236:239], v[68:71], v[12:15]\n"
      "ds_read_b64_tr_b16 v[240:241], v114 offset:12672\n"
      "s_waitcnt lgkmcnt(3)\n"
      "v_mfma_f32_16x16x32_bf16 v[16:19], v[224:227], v[72:75], v[16:19]\n"
      "ds_read_b128 v[68:71], v126 offset:32576\n"
      "ds_read_b64_tr_b16 v[242:243], v114 offset:13184\n"
      "v_mfma_f32_16x16x32_bf16 v[20:23], v[228:231], v[72:75], v[20:23]\n"
      "ds_read_b64_tr_b16 v[244:245], v114 offset:12680\n"
      "v_mfma_f32_16x16x32_bf16 v[116:119], v[232:235], v[72:75], v[116:119]\n"
      "ds_read_b64_tr_b16 v[246:247], v114 offset:13192\n"
      "v_mfma_f32_16x16x32_bf16 v[120:123], v[236:239], v[72:75], v[120:123]\n"
      "ds_read_b64_tr_b16 v[248:249], v115 offset:12672\n"
      "s_waitcnt lgkmcnt(7)\n"
      "v_mfma_f32_16x16x32_bf16 v[48:51], v[224:227], v[76:79], v[48:51]\n"
      "ds_read_b128 v[72:75], v98 offset:27392\n"
      "ds_read_b64_tr_b16 v[250:251], v115 offset:13184\n"
      "v_mfma_f32_16x16x32_bf16 v[52:55], v[228:231], v[76:79], v[52:55]\n"
      "ds_read_b64_tr_b16 v[252:253], v115 offset:12680\n"
      "v_mfma_f32_16x16x32_bf16 v[56:59], v[232:235], v[76:79], v[56:59]\n"
      "ds_read_b64_tr_b16 v[254:255], v115 offset:13192\n"
      "v_mfma_f32_16x16x32_bf16 v[60:63], v[236:239], v[76:79], v[60:63]\n"
      "ds_read_b128 v[76:79], v124 offset:11264\n"
      "s_waitcnt lgkmcnt(8)\n"
      "v_mfma_f32_16x16x32_bf16 v[80:83], v[240:243], v[64:67], v[80:83]\n"
      "s_waitcnt lgkmcnt(6)\n"
      "v_mfma_f32_16x16x32_bf16 v[84:87], v[244:247], v[64:67], v[84:87]\n"
      "s_waitcnt lgkmcnt(3)\n"
      "v_mfma_f32_16x16x32_bf16 v[88:91], v[248:251], v[64:67], v[88:91]\n"
      "s_waitcnt lgkmcnt(1)\n"
      "v_mfma_f32_16x16x32_bf16 v[92:95], v[252:255], v[64:67], v[92:95]\n"
      "v_mfma_f32_16x16x32_bf16 v[0:3], v[240:243], v[68:71], v[0:3]\n"
      "v_mfma_f32_16x16x32_bf16 v[4:7], v[244:247], v[68:71], v[4:7]\n"
      "v_mfma_f32_16x16x32_bf16 v[8:11], v[248:251], v[68:71], v[8:11]\n"
      "v_mul_f32 v80, v108, v80\n"
      "v_mul_f32 v81, v108, v81\n"
      "v_mfma_f32_16x16x32_bf16 v[12:15], v[252:255], v[68:71], v[12:15]\n"
      "v_cvt_pk_bf16_f32 v80, v80, v81\n"
      "v_mul_f32 v82, v108, v82\n"
      "v_mfma_f32_16x16x32_bf16 v[16:19], v[240:243], v[72:75], v[16:19]\n"
      "v_mul_f32 v83, v108, v83\n"
      "v_cvt_pk_bf16_f32 v81, v82, v83\n"
      "v_mfma_f32_16x16x32_bf16 v[20:23], v[244:247], v[72:75], v[20:23]\n"
      "v_mul_f32 v84, v108, v84\n"
      "v_mul_f32 v85, v108, v85\n"
      "v_mfma_f32_16x16x32_bf16 v[116:119], v[248:251], v[72:75], v[116:119]\n"
      "v_cvt_pk_bf16_f32 v82, v84, v85\n"
      "v_mul_f32 v86, v108, v86\n"
      "v_mfma_f32_16x16x32_bf16 v[120:123], v[252:255], v[72:75], v[120:123]\n"
      "v_mul_f32 v87, v108, v87\n"
      "v_cvt_pk_bf16_f32 v83, v86, v87\n"
      "s_waitcnt lgkmcnt(0)\n"
      "v_mfma_f32_16x16x32_bf16 v[48:51], v[240:243], v[76:79], v[48:51]\n"
      "ds_write_b128 v100, v[80:83]\n"
      "v_mfma_f32_16x16x32_bf16 v[52:55], v[244:247], v[76:79], v[52:55]\n"
      "v_mul_f32 v88, v108, v88\n"
      "v_mul_f32 v89, v108, v89\n"
      "v_mfma_f32_16x16x32_bf16 v[56:59], v[248:251], v[76:79], v[56:59]\n"
      "v_cvt_pk_bf16_f32 v84, v88, v89\n"
      "v_mul_f32 v90, v108, v90\n"
      "v_mfma_f32_16x16x32_bf16 v[60:63], v[252:255], v[76:79], v[60:63]\n"
      "v_mul_f32 v91, v108, v91\n"
      "v_cvt_pk_bf16_f32 v85, v90, v91\n"
      "v_mul_f32 v92, v108, v92\n"
      "v_mul_f32 v93, v108, v93\n"
      "v_cvt_pk_bf16_f32 v86, v92, v93\n"
      "v_mul_f32 v94, v108, v94\n"
      "v_mul_f32 v95, v108, v95\n"
      "v_cvt_pk_bf16_f32 v87, v94, v95\n"
      "ds_write_b128 v100, v[84:87] offset:16\n"
      "ds_read_b128 v[88:91], v101\n"
      "ds_read_b128 v[92:95], v101 offset:1152\n"
      "v_mul_f32 v0, v109, v0\n"
      "v_mul_f32 v1, v109, v1\n"
      "v_cvt_pk_bf16_f32 v0, v0, v1\n"
      "v_mul_f32 v2, v109, v2\n"
      "v_mul_f32 v3, v109, v3\n"
      "v_cvt_pk_bf16_f32 v1, v2, v3\n"
      "v_mul_f32 v4, v109, v4\n"
      "v_mul_f32 v5, v109, v5\n"
      "v_cvt_pk_bf16_f32 v2, v4, v5\n"
      "v_mul_f32 v6, v109, v6\n"
      "v_mul_f32 v7, v109, v7\n"
      "v_cvt_pk_bf16_f32 v3, v6, v7\n"
      "ds_write_b128 v100, v[0:3]\n"
      "s_mov_b32 s60, 8192\n"
      "s_mov_b32 s61, 12288\n"
      "s_waitcnt lgkmcnt(2)\n"
      "buffer_store_dwordx4 v[88:91], v103, s[48:51], s60 offen sc1\n"
      "s_waitcnt lgkmcnt(1)\n"
      "buffer_store_dwordx4 v[92:95], v103, s[48:51], s61 offen sc1\n"
      "v_mul_f32 v8, v109, v8\n"
      "v_mul_f32 v9, v109, v9\n"
      "v_cvt_pk_bf16_f32 v4, v8, v9\n"
      "v_mul_f32 v10, v109, v10\n"
      "v_mul_f32 v11, v109, v11\n"
      "v_cvt_pk_bf16_f32 v5, v10, v11\n"
      "v_mul_f32 v12, v109, v12\n"
      "v_mul_f32 v13, v109, v13\n"
      "v_cvt_pk_bf16_f32 v6, v12, v13\n"
      "v_mul_f32 v14, v109, v14\n"
      "v_mul_f32 v15, v109, v15\n"
      "v_cvt_pk_bf16_f32 v7, v14, v15\n"
      "ds_write_b128 v100, v[4:7] offset:16\n"
      "ds_read_b128 v[8:11], v101\n"
      "ds_read_b128 v[12:15], v101 offset:1152\n"
      "v_mul_f32 v16, v96, v16\n"
      "v_mul_f32 v17, v96, v17\n"
      "v_cvt_pk_bf16_f32 v16, v16, v17\n"
      "v_mul_f32 v18, v96, v18\n"
      "v_mul_f32 v19, v96, v19\n"
      "v_cvt_pk_bf16_f32 v17, v18, v19\n"
      "v_mul_f32 v20, v96, v20\n"
      "v_mul_f32 v21, v96, v21\n"
      "v_cvt_pk_bf16_f32 v18, v20, v21\n"
      "v_mul_f32 v22, v96, v22\n"
      "v_mul_f32 v23, v96, v23\n"
      "v_cvt_pk_bf16_f32 v19, v22, v23\n"
      "ds_write_b128 v100, v[16:19]\n"
      "s_mov_b32 s60, 16384\n"
      "s_mov_b32 s61, 20480\n"
      "s_waitcnt lgkmcnt(2)\n"
      "buffer_store_dwordx4 v[8:11], v103, s[48:51], s60 offen sc1\n"
      "s_waitcnt lgkmcnt(1)\n"
      "buffer_store_dwordx4 v[12:15], v103, s[48:51], s61 offen sc1\n"
      "v_mul_f32 v116, v96, v116\n"
      "v_mul_f32 v117, v96, v117\n"
      "v_cvt_pk_bf16_f32 v20, v116, v117\n"
      "v_mul_f32 v118, v96, v118\n"
      "v_mul_f32 v119, v96, v119\n"
      "v_cvt_pk_bf16_f32 v21, v118, v119\n"
      "v_mul_f32 v120, v96, v120\n"
      "v_mul_f32 v121, v96, v121\n"
      "v_cvt_pk_bf16_f32 v22, v120, v121\n"
      "v_mul_f32 v122, v96, v122\n"
      "v_mul_f32 v123, v96, v123\n"
      "v_cvt_pk_bf16_f32 v23, v122, v123\n"
      "ds_write_b128 v100, v[20:23] offset:16\n"
      "ds_read_b128 v[116:119], v101\n"
      "ds_read_b128 v[120:123], v101 offset:1152\n"
      "s_mov_b32 s60, 24576\n"
      "s_mov_b32 s61, 28672\n"
      "s_waitcnt lgkmcnt(1)\n"
      "buffer_store_dwordx4 v[116:119], v103, s[48:51], s60 offen sc1\n"
      "s_waitcnt lgkmcnt(0)\n"
      "buffer_store_dwordx4 v[120:123], v103, s[48:51], s61 offen sc1\n"
      "s_mov_b32 s60, 0\n"
      "s_mov_b32 s61, 4096\n"
      "v_add_u32 v104, 384, v104\n"
      "v_add_u32 v96, 23232, v97\n"
      "s_waitcnt vmcnt(12)\n"
      "s_barrier\n"
      "ds_read_b128 v[64:67], v96\n"
      "ds_read_b128 v[68:71], v96 offset:64\n"
      "ds_read_b128 v[72:75], v96 offset:128\n"
      "ds_read_b128 v[76:79], v96 offset:192\n"
      "s_waitcnt lgkmcnt(3)\n"
      "v_mfma_f32_16x16x32_bf16 v[0:3], a[0:3], v[64:67], 0\n"
      "ds_read_b128 v[80:83], v96 offset:256\n"
      "s_waitcnt lgkmcnt(3)\n"
      "v_mfma_f32_16x16x32_bf16 v[0:3], a[4:7], v[68:71], v[0:3]\n"
      "v_mfma_f32_16x16x32_bf16 v[4:7], a[36:39], v[68:71], 0\n"
      "ds_read_b128 v[84:87], v96 offset:320\n"
      "v_mul_f32 v48, v107, v48\n"
      "v_mfma_f32_16x16x32_bf16 v[4:7], a[32:35], v[64:67], v[4:7]\n"
      "v_mul_f32 v49, v107, v49\n"
      "v_cvt_pk_bf16_f32 v48, v48, v49\n"
      "v_mfma_f32_16x16x32_bf16 v[8:11], a[64:67], v[64:67], 0\n"
      "v_mul_f32 v50, v107, v50\n"
      "v_mul_f32 v51, v107, v51\n"
      "v_mfma_f32_16x16x32_bf16 v[8:11], a[68:71], v[68:71], v[8:11]\n"
      "v_cvt_pk_bf16_f32 v49, v50, v51\n"
      "v_mul_f32 v52, v107, v52\n"
      "v_mfma_f32_16x16x32_bf16 v[12:15], a[100:103], v[68:71], 0\n"
      "v_mul_f32 v53, v107, v53\n"
      "v_cvt_pk_bf16_f32 v50, v52, v53\n"
      "v_mfma_f32_16x16x32_bf16 v[12:15], a[96:99], v[64:67], v[12:15]\n"
      "v_mul_f32 v54, v107, v54\n"
      "v_mul_f32 v55, v107, v55\n"
      "v_mfma_f32_16x16x32_bf16 v[16:19], a[128:131], v[64:67], 0\n"
      "v_cvt_pk_bf16_f32 v51, v54, v55\n"
      "v_mul_f32 v56, v107, v56\n"
      "v_mfma_f32_16x16x32_bf16 v[16:19], a[132:135], v[68:71], v[16:19]\n"
      "ds_write_b128 v100, v[48:51]\n"
      "v_mfma_f32_16x16x32_bf16 v[20:23], a[164:167], v[68:71], 0\n"
      "v_mul_f32 v57, v107, v57\n"
      "v_cvt_pk_bf16_f32 v52, v56, v57\n"
      "v_mfma_f32_16x16x32_bf16 v[20:23], a[160:163], v[64:67], v[20:23]\n"
      "ds_read_b32 v109, v104 offset:64\n"
      "v_mul_f32 v58, v107, v58\n"
      "s_waitcnt lgkmcnt(5)\n"
      "v_mfma_f32_16x16x32_bf16 v[0:3], a[8:11], v[72:75], v[0:3]\n"
      "ds_read_b128 v[88:91], v96 offset:384\n"
      "v_mul_f32 v59, v107, v59\n"
      "s_waitcnt lgkmcnt(5)\n"
      "v_mfma_f32_16x16x32_bf16 v[0:3], a[12:15], v[76:79], v[0:3]\n"
      "s_add_u32 m0, s53, 4224\n"
      "v_cvt_pk_bf16_f32 v53, v58, v59\n"
      "v_mul_f32 v60, v107, v60\n"
      "v_mfma_f32_16x16x32_bf16 v[4:7], a[44:47], v[76:79], v[4:7]\n"
      "ds_read_b128 v[92:95], v96 offset:448\n"
      "v_mul_f32 v61, v107, v61\n"
      "v_mfma_f32_16x16x32_bf16 v[4:7], a[40:43], v[72:75], v[4:7]\n"
      "ds_read_b32 v112, v104 offset:1536\n"
      "v_cvt_pk_bf16_f32 v54, v60, v61\n"
      "v_mfma_f32_16x16x32_bf16 v[8:11], a[72:75], v[72:75], v[8:11]\n"
      "v_mul_f32 v62, v107, v62\n"
      "v_mul_f32 v63, v107, v63\n"
      "v_mfma_f32_16x16x32_bf16 v[8:11], a[76:79], v[76:79], v[8:11]\n"
      "s_waitcnt lgkmcnt(3)\n"
      "v_cvt_pk_bf16_f32 v55, v62, v63\n"
      "v_fma_f32 v105, v109, s56, v111\n"
      "v_mfma_f32_16x16x32_bf16 v[12:15], a[108:111], v[76:79], v[12:15]\n"
      "ds_write_b128 v100, v[52:55] offset:16\n"
      "v_mfma_f32_16x16x32_bf16 v[12:15], a[104:107], v[72:75], v[12:15]\n"
      "ds_read_b128 v[56:59], v101\n"
      "v_fma_f32 v24, v24, s55, v105\n"
      "v_mfma_f32_16x16x32_bf16 v[16:19], a[136:139], v[72:75], v[16:19]\n"
      "v_exp_f32 v24, v24\n"
      "v_mfma_f32_16x16x32_bf16 v[16:19], a[140:143], v[76:79], v[16:19]\n"
      "ds_read_b128 v[60:63], v101 offset:1152\n"
      "v_fma_f32 v25, v25, s55, v105\n"
      "v_mfma_f32_16x16x32_bf16 v[20:23], a[172:175], v[76:79], v[20:23]\n"
      "s_mov_b32 s59, 25344\n"
      "v_exp_f32 v25, v25\n"
      "v_mfma_f32_16x16x32_bf16 v[20:23], a[168:171], v[72:75], v[20:23]\n"
      "s_waitcnt lgkmcnt(3)\n"
      "v_add_f32 v106, v24, v25\n"
      "v_cvt_pk_bf16_f32 v24, v24, v25\n"
      "v_mfma_f32_16x16x32_bf16 v[0:3], a[16:19], v[80:83], v[0:3]\n"
      "ds_read_b128 v[64:67], v98 offset:3072\n"
      "v_fma_f32 v26, v26, s55, v105\n"
      "v_mfma_f32_16x16x32_bf16 v[0:3], a[20:23], v[84:87], v[0:3]\n"
      "v_exp_f32 v26, v26\n"
      "v_mfma_f32_16x16x32_bf16 v[4:7], a[52:55], v[84:87], v[4:7]\n"
      "ds_read_b128 v[68:71], v98 offset:4096\n"
      "v_add_f32 v106, v106, v26\n"
      "v_mfma_f32_16x16x32_bf16 v[4:7], a[48:51], v[80:83], v[4:7]\n"
      "s_waitcnt lgkmcnt(2)\n"
      "v_fma_f32 v27, v27, s55, v105\n"
      "v_fma_f32 v28, v28, s55, v105\n"
      "v_mfma_f32_16x16x32_bf16 v[8:11], a[80:83], v[80:83], v[8:11]\n"
      "buffer_store_dwordx4 v[56:59], v103, s[48:51], s60 offen sc1\n"
      "v_cvt_f32_u32 v112, v112\n"
      "v_mfma_f32_16x16x32_bf16 v[8:11], a[84:87], v[84:87], v[8:11]\n"
      "s_mov_b32 s60, 32768\n"
      "v_exp_f32 v27, v27\n"
      "v_mfma_f32_16x16x32_bf16 v[12:15], a[116:119], v[84:87], v[12:15]\n"
      "v_add_f32 v106, v106, v27\n"
      "v_cvt_pk_bf16_f32 v25, v26, v27\n"
      "v_mfma_f32_16x16x32_bf16 v[12:15], a[112:115], v[80:83], v[12:15]\n"
      "v_exp_f32 v28, v28\n"
      "v_mfma_f32_16x16x32_bf16 v[16:19], a[144:147], v[80:83], v[16:19]\n"
      "v_add_f32 v106, v106, v28\n"
      "v_fma_f32 v29, v29, s55, v105\n"
      "v_mfma_f32_16x16x32_bf16 v[16:19], a[148:151], v[84:87], v[16:19]\n"
      "buffer_store_dwordx4 v[60:63], v103, s[48:51], s61 offen sc1\n"
      "v_fma_f32 v30, v30, s55, v105\n"
      "v_mfma_f32_16x16x32_bf16 v[20:23], a[180:183], v[84:87], v[20:23]\n"
      "s_mov_b32 s61, 36864\n"
      "v_rcp_f32 v107, v112\n"
      "v_mfma_f32_16x16x32_bf16 v[20:23], a[176:179], v[80:83], v[20:23]\n"
      "v_exp_f32 v29, v29\n"
      "v_mfma_f32_16x16x32_bf16 v[0:3], a[24:27], v[88:91], v[0:3]\n"
      "ds_read_b128 v[72:75], v98 offset:5120\n"
      "v_add_f32 v106, v106, v29\n"
      "v_mfma_f32_16x16x32_bf16 v[0:3], a[28:31], v[92:95], v[0:3]\n"
      "v_fma_f32 v31, v31, s55, v105\n"
      "v_cvt_pk_bf16_f32 v26, v28, v29\n"
      "v_mfma_f32_16x16x32_bf16 v[4:7], a[60:63], v[92:95], v[4:7]\n"
      "ds_read_b128 v[76:79], v98 offset:11520\n"
      "v_fma_f32 v32, v32, s55, v105\n"
      "v_mfma_f32_16x16x32_bf16 v[4:7], a[56:59], v[88:91], v[4:7]\n"
      "v_exp_f32 v30, v30\n"
      "v_mfma_f32_16x16x32_bf16 v[8:11], a[88:91], v[88:91], v[8:11]\n"
      "v_add_f32 v106, v106, v30\n"
      "v_fma_f32 v33, v33, s55, v105\n"
      "v_mfma_f32_16x16x32_bf16 v[8:11], a[92:95], v[92:95], v[8:11]\n"
      "v_exp_f32 v31, v31\n"
      "v_mfma_f32_16x16x32_bf16 v[12:15], a[124:127], v[92:95], v[12:15]\n"
      "v_add_f32 v106, v106, v31\n"
      "v_cvt_pk_bf16_f32 v27, v30, v31\n"
      "v_mfma_f32_16x16x32_bf16 v[12:15], a[120:123], v[88:91], v[12:15]\n"
      "ds_write_b128 v99, v[24:27]\n"
      "v_mfma_f32_16x16x32_bf16 v[16:19], a[152:155], v[88:91], v[16:19]\n"
      "s_waitcnt lgkmcnt(3)\n"
      "v_exp_f32 v32, v32\n"
      "v_mfma_f32_16x16x32_bf16 v[16:19], a[156:159], v[92:95], v[16:19]\n"
      "v_add_f32 v106, v106, v32\n"
      "v_fma_f32 v34, v34, s55, v105\n"
      "v_mfma_f32_16x16x32_bf16 v[20:23], a[188:191], v[92:95], v[20:23]\n"
      "v_exp_f32 v33, v33\n"
      "v_mfma_f32_16x16x32_bf16 v[20:23], a[184:187], v[88:91], v[20:23]\n"
      "v_add_f32 v106, v106, v33\n"
      "v_cvt_pk_bf16_f32 v32, v32, v33\n"
      "v_mfma_f32_16x16x32_bf16 v[48:51], a[192:195], v[64:67], 0\n"
      "ds_read_b128 v[80:83], v98 offset:12544\n"
      "v_max3_f32 v108, v0, v1, v2\n"
      "v_mfma_f32_16x16x32_bf16 v[48:51], a[208:211], v[68:71], v[48:51]\n"
      "buffer_load_dwordx4 v102, s[36:39], s57 offen nt lds\n"
      "v_mfma_f32_16x16x32_bf16 v[52:55], a[212:215], v[68:71], 0\n"
      "ds_read_b128 v[84:87], v98 offset:13568\n"
      "v_fma_f32 v35, v35, s55, v105\n"
      "v_mfma_f32_16x16x32_bf16 v[52:55], a[196:199], v[64:67], v[52:55]\n"
      "s_add_u32 m0, m0, 544\n"
      "v_exp_f32 v34, v34\n"
      "v_mfma_f32_16x16x32_bf16 v[56:59], a[200:203], v[64:67], 0\n"
      "s_waitcnt lgkmcnt(3)\n"
      "v_add_f32 v106, v106, v34\n"
      "v_max3_f32 v108, v108, v3, v4\n"
      "v_mfma_f32_16x16x32_bf16 v[56:59], a[216:219], v[68:71], v[56:59]\n"
      "buffer_load_dwordx4 v102, s[36:39], s57 offen offset:512 nt lds\n"
      "v_mfma_f32_16x16x32_bf16 v[60:63], a[220:223], v[68:71], 0\n"
      "s_add_u32 s57, s57, 8192\n"
      "v_exp_f32 v35, v35\n"
      "v_mfma_f32_16x16x32_bf16 v[60:63], a[204:207], v[64:67], v[60:63]\n"
      "v_add_f32 v106, v106, v35\n"
      "v_cvt_pk_bf16_f32 v33, v34, v35\n"
      "v_mfma_f32_16x16x32_bf16 v[48:51], a[224:227], v[72:75], v[48:51]\n"
      "ds_read_b128 v[88:91], v98 offset:19968\n"
      "v_fma_f32 v36, v36, s55, v105\n"
      "v_mfma_f32_16x16x32_bf16 v[48:51], a[240:243], v[76:79], v[48:51]\n"
      "v_max3_f32 v108, v108, v5, v6\n"
      "v_max3_f32 v108, v108, v7, v8\n"
      "v_mfma_f32_16x16x32_bf16 v[52:55], a[244:247], v[76:79], v[52:55]\n"
      "ds_read_b128 v[92:95], v98 offset:20992\n"
      "v_fma_f32 v37, v37, s55, v105\n"
      "v_mfma_f32_16x16x32_bf16 v[52:55], a[228:231], v[72:75], v[52:55]\n"
      "v_exp_f32 v36, v36\n"
      "v_mfma_f32_16x16x32_bf16 v[56:59], a[232:235], v[72:75], v[56:59]\n"
      "s_waitcnt lgkmcnt(2)\n"
      "v_add_f32 v106, v106, v36\n"
      "v_mfma_f32_16x16x32_bf16 v[56:59], a[248:251], v[76:79], v[56:59]\n"
      "v_exp_f32 v37, v37\n"
      "v_mfma_f32_16x16x32_bf16 v[60:63], a[252:255], v[76:79], v[60:63]\n"
      "v_add_f32 v106, v106, v37\n"
      "v_cvt_pk_bf16_f32 v34, v36, v37\n"
      "v_mfma_f32_16x16x32_bf16 v[60:63], a[236:239], v[72:75], v[60:63]\n"
      "v_fma_f32 v38, v38, s55, v105\n"
      "v_max3_f32 v108, v108, v9, v10\n"
      "v_mfma_f32_16x16x32_bf16 v[48:51], v[128:131], v[80:83], v[48:51]\n"
      "ds_read_b128 v[64:67], v98 offset:22016\n"
      "v_max3_f32 v108, v108, v11, v12\n"
      "v_mfma_f32_16x16x32_bf16 v[48:51], v[144:147], v[84:87], v[48:51]\n"
      "v_exp_f32 v38, v38\n"
      "v_mfma_f32_16x16x32_bf16 v[52:55], v[148:151], v[84:87], v[52:55]\n"
      "ds_read_b128 v[68:71], v98 offset:28416\n"
      "v_add_f32 v106, v106, v38\n"
      "v_mfma_f32_16x16x32_bf16 v[52:55], v[132:135], v[80:83], v[52:55]\n"
      "v_fma_f32 v39, v39, s55, v105\n"
      "v_max3_f32 v108, v108, v13, v14\n"
      "v_mfma_f32_16x16x32_bf16 v[56:59], v[136:139], v[80:83], v[56:59]\n"
      "s_waitcnt lgkmcnt(2)\n"
      "v_exp_f32 v39, v39\n"
      "v_mfma_f32_16x16x32_bf16 v[56:59], v[152:155], v[84:87], v[56:59]\n"
      "v_add_f32 v106, v106, v39\n"
      "v_cvt_pk_bf16_f32 v35, v38, v39\n"
      "v_mfma_f32_16x16x32_bf16 v[60:63], v[156:159], v[84:87], v[60:63]\n"
      "ds_write_b128 v99, v[32:35] offset:1024\n"
      "s_waitcnt vmcnt(14)\n"
      "s_barrier\n"
      "v_mfma_f32_16x16x32_bf16 v[60:63], v[140:143], v[80:83], v[60:63]\n"
      "v_add_u32 v96, s59, v97\n"
      "v_fma_f32 v40, v40, s55, v105\n"
      "v_mfma_f32_16x16x32_bf16 v[48:51], v[160:163], v[88:91], v[48:51]\n"
      "ds_read_b128 v[72:75], v98 offset:29440\n"
      "v_max3_f32 v108, v108, v15, v16\n"
      "v_mfma_f32_16x16x32_bf16 v[48:51], v[176:179], v[92:95], v[48:51]\n"
      "v_exp_f32 v40, v40\n"
      "v_mfma_f32_16x16x32_bf16 v[52:55], v[180:183], v[92:95], v[52:55]\n"
      "ds_read_b128 v[76:79], v98 offset:30464\n"
      "v_add_f32 v106, v106, v40\n"
      "v_mfma_f32_16x16x32_bf16 v[52:55], v[164:167], v[88:91], v[52:55]\n"
      "v_fma_f32 v41, v41, s55, v105\n"
      "v_max3_f32 v108, v108, v17, v18\n"
      "v_mfma_f32_16x16x32_bf16 v[56:59], v[168:171], v[88:91], v[56:59]\n"
      "s_waitcnt lgkmcnt(3)\n"
      "v_exp_f32 v41, v41\n"
      "v_mfma_f32_16x16x32_bf16 v[56:59], v[184:187], v[92:95], v[56:59]\n"
      "v_add_f32 v106, v106, v41\n"
      "v_cvt_pk_bf16_f32 v40, v40, v41\n"
      "v_mfma_f32_16x16x32_bf16 v[60:63], v[188:191], v[92:95], v[60:63]\n"
      "v_fma_f32 v42, v42, s55, v105\n"
      "v_max3_f32 v108, v108, v19, v20\n"
      "v_mfma_f32_16x16x32_bf16 v[60:63], v[172:175], v[88:91], v[60:63]\n"
      "v_exp_f32 v42, v42\n"
      "v_mfma_f32_16x16x32_bf16 v[48:51], v[192:195], v[64:67], v[48:51]\n"
      "ds_read_b128 v[80:83], v96\n"
      "v_add_f32 v106, v106, v42\n"
      "v_mfma_f32_16x16x32_bf16 v[48:51], v[208:211], v[68:71], v[48:51]\n"
      "v_fma_f32 v43, v43, s55, v105\n"
      "v_max3_f32 v108, v108, v21, v22\n"
      "v_mfma_f32_16x16x32_bf16 v[52:55], v[212:215], v[68:71], v[52:55]\n"
      "ds_read_b128 v[84:87], v96 offset:64\n"
      "v_fma_f32 v44, v44, s55, v105\n"
      "v_mfma_f32_16x16x32_bf16 v[52:55], v[196:199], v[64:67], v[52:55]\n"
      "v_exp_f32 v43, v43\n"
      "v_mfma_f32_16x16x32_bf16 v[56:59], v[200:203], v[64:67], v[56:59]\n"
      "s_waitcnt lgkmcnt(2)\n"
      "v_add_f32 v106, v106, v43\n"
      "v_cvt_pk_bf16_f32 v41, v42, v43\n"
      "v_mfma_f32_16x16x32_bf16 v[56:59], v[216:219], v[68:71], v[56:59]\n"
      "v_exp_f32 v44, v44\n"
      "v_mfma_f32_16x16x32_bf16 v[60:63], v[220:223], v[68:71], v[60:63]\n"
      "v_add_f32 v106, v106, v44\n"
      "v_max_f32 v108, v108, v23\n"
      "v_mfma_f32_16x16x32_bf16 v[60:63], v[204:207], v[64:67], v[60:63]\n"
      "ds_max_f32 v104, v108 offset:128\n"
      "v_mfma_f32_16x16x32_bf16 v[48:51], v[224:227], v[72:75], v[48:51]\n"
      "ds_read_b128 v[88:91], v96 offset:128\n"
      "v_fma_f32 v45, v45, s55, v105\n"
      "v_mfma_f32_16x16x32_bf16 v[48:51], v[240:243], v[76:79], v[48:51]\n"
      "v_exp_f32 v45, v45\n"
      "v_mfma_f32_16x16x32_bf16 v[52:55], v[244:247], v[76:79], v[52:55]\n"
      "ds_read_b128 v[92:95], v96 offset:192\n"
      "v_add_f32 v106, v106, v45\n"
      "v_mfma_f32_16x16x32_bf16 v[52:55], v[228:231], v[72:75], v[52:55]\n"
      "v_fma_f32 v46, v46, s55, v105\n"
      "v_cvt_pk_bf16_f32 v42, v44, v45\n"
      "v_mfma_f32_16x16x32_bf16 v[56:59], v[232:235], v[72:75], v[56:59]\n"
      "s_waitcnt lgkmcnt(3)\n"
      "v_exp_f32 v46, v46\n"
      "v_mfma_f32_16x16x32_bf16 v[56:59], v[248:251], v[76:79], v[56:59]\n"
      "v_add_f32 v106, v106, v46\n"
      "v_fma_f32 v47, v47, s55, v105\n"
      "v_mfma_f32_16x16x32_bf16 v[60:63], v[252:255], v[76:79], v[60:63]\n"
      "v_exp_f32 v47, v47\n"
      "v_mfma_f32_16x16x32_bf16 v[60:63], v[236:239], v[72:75], v[60:63]\n"
      "v_add_f32 v106, v106, v47\n"
      "v_cvt_pk_bf16_f32 v43, v46, v47\n"
      "s_bitcmp1_b32 s52, 1\n"
      "s_cbranch_scc1 7f\n"
      "v_mfma_f32_16x16x32_bf16 v[24:27], a[0:3], v[80:83], 0\n"
      "ds_read_b128 v[64:67], v96 offset:256\n"
      "v_cvt_u32_f32 v110, v106\n"
      "v_mfma_f32_16x16x32_bf16 v[24:27], a[4:7], v[84:87], v[24:27]\n"
      "ds_write_b128 v99, v[40:43] offset:2048\n"
      "ds_add_u32 v104, v110 offset:1600\n"
      "v_mfma_f32_16x16x32_bf16 v[28:31], a[36:39], v[84:87], 0\n"
      "ds_read_b128 v[68:71], v96 offset:320\n"
      "v_mul_f32 v48, v107, v48\n"
      "v_mfma_f32_16x16x32_bf16 v[28:31], a[32:35], v[80:83], v[28:31]\n"
      "v_mul_f32 v49, v107, v49\n"
      "v_cvt_pk_bf16_f32 v48, v48, v49\n"
      "v_mfma_f32_16x16x32_bf16 v[32:35], a[64:67], v[80:83], 0\n"
      "v_mul_f32 v50, v107, v50\n"
      "v_mul_f32 v51, v107, v51\n"
      "v_mfma_f32_16x16x32_bf16 v[32:35], a[68:71], v[84:87], v[32:35]\n"
      "v_cvt_pk_bf16_f32 v49, v50, v51\n"
      "v_mul_f32 v52, v107, v52\n"
      "v_mfma_f32_16x16x32_bf16 v[36:39], a[100:103], v[84:87], 0\n"
      "s_waitcnt vmcnt(12) lgkmcnt(1)\n"
      "s_barrier\n"
      "v_mul_f32 v53, v107, v53\n"
      "v_cvt_pk_bf16_f32 v50, v52, v53\n"
      "v_mfma_f32_16x16x32_bf16 v[36:39], a[96:99], v[80:83], v[36:39]\n"
      "v_mul_f32 v54, v107, v54\n"
      "v_mul_f32 v55, v107, v55\n"
      "v_mfma_f32_16x16x32_bf16 v[40:43], a[128:131], v[80:83], 0\n"
      "v_cvt_pk_bf16_f32 v51, v54, v55\n"
      "v_mul_f32 v56, v107, v56\n"
      "v_mfma_f32_16x16x32_bf16 v[40:43], a[132:135], v[84:87], v[40:43]\n"
      "ds_write_b128 v100, v[48:51]\n"
      "v_mfma_f32_16x16x32_bf16 v[44:47], a[164:167], v[84:87], 0\n"
      "v_mul_f32 v57, v107, v57\n"
      "v_cvt_pk_bf16_f32 v52, v56, v57\n"
      "v_mfma_f32_16x16x32_bf16 v[44:47], a[160:163], v[80:83], v[44:47]\n"
      "ds_read_b32 v109, v104 offset:128\n"
      "v_mul_f32 v58, v107, v58\n"
      "v_mfma_f32_16x16x32_bf16 v[24:27], a[8:11], v[88:91], v[24:27]\n"
      "ds_read_b128 v[72:75], v96 offset:384\n"
      "v_mul_f32 v59, v107, v59\n"
      "v_mfma_f32_16x16x32_bf16 v[24:27], a[12:15], v[92:95], v[24:27]\n"
      "s_add_u32 m0, s53, 6336\n"
      "v_cvt_pk_bf16_f32 v53, v58, v59\n"
      "v_mul_f32 v60, v107, v60\n"
      "v_mfma_f32_16x16x32_bf16 v[28:31], a[44:47], v[92:95], v[28:31]\n"
      "ds_read_b128 v[76:79], v96 offset:448\n"
      "v_mul_f32 v61, v107, v61\n"
      "v_mfma_f32_16x16x32_bf16 v[28:31], a[40:43], v[88:91], v[28:31]\n"
      "ds_read_b32 v112, v104 offset:1600\n"
      "v_cvt_pk_bf16_f32 v54, v60, v61\n"
      "v_mfma_f32_16x16x32_bf16 v[32:35], a[72:75], v[88:91], v[32:35]\n"
      "v_mul_f32 v62, v107, v62\n"
      "v_mul_f32 v63, v107, v63\n"
      "v_mfma_f32_16x16x32_bf16 v[32:35], a[76:79], v[92:95], v[32:35]\n"
      "s_waitcnt lgkmcnt(3)\n"
      "v_cvt_pk_bf16_f32 v55, v62, v63\n"
      "v_fma_f32 v105, v109, s56, v111\n"
      "v_mfma_f32_16x16x32_bf16 v[36:39], a[108:111], v[92:95], v[36:39]\n"
      "ds_write_b128 v100, v[52:55] offset:16\n"
      "v_mfma_f32_16x16x32_bf16 v[36:39], a[104:107], v[88:91], v[36:39]\n"
      "ds_read_b128 v[56:59], v101\n"
      "v_fma_f32 v0, v0, s55, v105\n"
      "v_mfma_f32_16x16x32_bf16 v[40:43], a[136:139], v[88:91], v[40:43]\n"
      "v_exp_f32 v0, v0\n"
      "v_mfma_f32_16x16x32_bf16 v[40:43], a[140:143], v[92:95], v[40:43]\n"
      "ds_read_b128 v[60:63], v101 offset:1152\n"
      "v_fma_f32 v1, v1, s55, v105\n"
      "v_mfma_f32_16x16x32_bf16 v[44:47], a[172:175], v[92:95], v[44:47]\n"
      "s_mov_b32 s59, 0\n"
      "v_exp_f32 v1, v1\n"
      "v_mfma_f32_16x16x32_bf16 v[44:47], a[168:171], v[88:91], v[44:47]\n"
      "s_waitcnt lgkmcnt(3)\n"
      "v_add_f32 v106, v0, v1\n"
      "v_cvt_pk_bf16_f32 v0, v0, v1\n"
      "v_mfma_f32_16x16x32_bf16 v[24:27], a[16:19], v[64:67], v[24:27]\n"
      "ds_read_b128 v[80:83], v98\n"
      "v_fma_f32 v2, v2, s55, v105\n"
      "v_mfma_f32_16x16x32_bf16 v[24:27], a[20:23], v[68:71], v[24:27]\n"
      "v_exp_f32 v2, v2\n"
      "v_mfma_f32_16x16x32_bf16 v[28:31], a[52:55], v[68:71], v[28:31]\n"
      "ds_read_b128 v[84:87], v98 offset:1024\n"
      "v_add_f32 v106, v106, v2\n"
      "v_mfma_f32_16x16x32_bf16 v[28:31], a[48:51], v[64:67], v[28:31]\n"
      "s_waitcnt lgkmcnt(2)\n"
      "v_fma_f32 v3, v3, s55, v105\n"
      "v_fma_f32 v4, v4, s55, v105\n"
      "v_mfma_f32_16x16x32_bf16 v[32:35], a[80:83], v[64:67], v[32:35]\n"
      "buffer_store_dwordx4 v[56:59], v103, s[48:51], s60 offen sc1\n"
      "v_cvt_f32_u32 v112, v112\n"
      "v_mfma_f32_16x16x32_bf16 v[32:35], a[84:87], v[68:71], v[32:35]\n"
      "s_add_u32 s60, s60, 8192\n"
      "v_exp_f32 v3, v3\n"
      "v_mfma_f32_16x16x32_bf16 v[36:39], a[116:119], v[68:71], v[36:39]\n"
      "v_add_f32 v106, v106, v3\n"
      "v_cvt_pk_bf16_f32 v1, v2, v3\n"
      "v_mfma_f32_16x16x32_bf16 v[36:39], a[112:115], v[64:67], v[36:39]\n"
      "v_exp_f32 v4, v4\n"
      "v_mfma_f32_16x16x32_bf16 v[40:43], a[144:147], v[64:67], v[40:43]\n"
      "v_add_f32 v106, v106, v4\n"
      "v_fma_f32 v5, v5, s55, v105\n"
      "v_mfma_f32_16x16x32_bf16 v[40:43], a[148:151], v[68:71], v[40:43]\n"
      "buffer_store_dwordx4 v[60:63], v103, s[48:51], s61 offen sc1\n"
      "v_fma_f32 v6, v6, s55, v105\n"
      "v_mfma_f32_16x16x32_bf16 v[44:47], a[180:183], v[68:71], v[44:47]\n"
      "s_add_u32 s61, s61, 8192\n"
      "v_rcp_f32 v107, v112\n"
      "v_mfma_f32_16x16x32_bf16 v[44:47], a[176:179], v[64:67], v[44:47]\n"
      "v_exp_f32 v5, v5\n"
      "v_mfma_f32_16x16x32_bf16 v[24:27], a[24:27], v[72:75], v[24:27]\n"
      "ds_read_b128 v[88:91], v98 offset:2048\n"
      "v_add_f32 v106, v106, v5\n"
      "v_mfma_f32_16x16x32_bf16 v[24:27], a[28:31], v[76:79], v[24:27]\n"
      "v_fma_f32 v7, v7, s55, v105\n"
      "v_cvt_pk_bf16_f32 v2, v4, v5\n"
      "v_mfma_f32_16x16x32_bf16 v[28:31], a[60:63], v[76:79], v[28:31]\n"
      "ds_read_b128 v[92:95], v98 offset:8448\n"
      "v_fma_f32 v8, v8, s55, v105\n"
      "v_mfma_f32_16x16x32_bf16 v[28:31], a[56:59], v[72:75], v[28:31]\n"
      "v_exp_f32 v6, v6\n"
      "v_mfma_f32_16x16x32_bf16 v[32:35], a[88:91], v[72:75], v[32:35]\n"
      "v_add_f32 v106, v106, v6\n"
      "v_fma_f32 v9, v9, s55, v105\n"
      "v_mfma_f32_16x16x32_bf16 v[32:35], a[92:95], v[76:79], v[32:35]\n"
      "v_exp_f32 v7, v7\n"
      "v_mfma_f32_16x16x32_bf16 v[36:39], a[124:127], v[76:79], v[36:39]\n"
      "v_add_f32 v106, v106, v7\n"
      "v_cvt_pk_bf16_f32 v3, v6, v7\n"
      "v_mfma_f32_16x16x32_bf16 v[36:39], a[120:123], v[72:75], v[36:39]\n"
      "ds_write_b128 v99, v[0:3] offset:3072\n"
      "v_mfma_f32_16x16x32_bf16 v[40:43], a[152:155], v[72:75], v[40:43]\n"
      "s_waitcnt lgkmcnt(3)\n"
      "v_exp_f32 v8, v8\n"
      "v_mfma_f32_16x16x32_bf16 v[40:43], a[156:159], v[76:79], v[40:43]\n"
      "v_add_f32 v106, v106, v8\n"
      "v_fma_f32 v10, v10, s55, v105\n"
      "v_mfma_f32_16x16x32_bf16 v[44:47], a[188:191], v[76:79], v[44:47]\n"
      "v_exp_f32 v9, v9\n"
      "v_mfma_f32_16x16x32_bf16 v[44:47], a[184:187], v[72:75], v[44:47]\n"
      "v_add_f32 v106, v106, v9\n"
      "v_cvt_pk_bf16_f32 v8, v8, v9\n"
      "v_mfma_f32_16x16x32_bf16 v[48:51], a[192:195], v[80:83], 0\n"
      "ds_read_b128 v[64:67], v98 offset:9472\n"
      "v_max3_f32 v108, v24, v25, v26\n"
      "v_mfma_f32_16x16x32_bf16 v[48:51], a[208:211], v[84:87], v[48:51]\n"
      "buffer_load_dwordx4 v102, s[36:39], s57 offen nt lds\n"
      "v_mfma_f32_16x16x32_bf16 v[52:55], a[212:215], v[84:87], 0\n"
      "ds_read_b128 v[68:71], v98 offset:10496\n"
      "v_fma_f32 v11, v11, s55, v105\n"
      "v_mfma_f32_16x16x32_bf16 v[52:55], a[196:199], v[80:83], v[52:55]\n"
      "s_add_u32 m0, m0, 544\n"
      "v_exp_f32 v10, v10\n"
      "v_mfma_f32_16x16x32_bf16 v[56:59], a[200:203], v[80:83], 0\n"
      "s_waitcnt lgkmcnt(3)\n"
      "v_add_f32 v106, v106, v10\n"
      "v_max3_f32 v108, v108, v27, v28\n"
      "v_mfma_f32_16x16x32_bf16 v[56:59], a[216:219], v[84:87], v[56:59]\n"
      "buffer_load_dwordx4 v102, s[36:39], s57 offen offset:512 nt lds\n"
      "v_mfma_f32_16x16x32_bf16 v[60:63], a[220:223], v[84:87], 0\n"
      "s_add_u32 s57, s57, 8192\n"
      "v_exp_f32 v11, v11\n"
      "v_mfma_f32_16x16x32_bf16 v[60:63], a[204:207], v[80:83], v[60:63]\n"
      "v_add_f32 v106, v106, v11\n"
      "v_cvt_pk_bf16_f32 v9, v10, v11\n"
      "v_mfma_f32_16x16x32_bf16 v[48:51], a[224:227], v[88:91], v[48:51]\n"
      "ds_read_b128 v[72:75], v98 offset:16896\n"
      "v_fma_f32 v12, v12, s55, v105\n"
      "v_mfma_f32_16x16x32_bf16 v[48:51], a[240:243], v[92:95], v[48:51]\n"
      "v_max3_f32 v108, v108, v29, v30\n"
      "v_max3_f32 v108, v108, v31, v32\n"
      "v_mfma_f32_16x16x32_bf16 v[52:55], a[244:247], v[92:95], v[52:55]\n"
      "ds_read_b128 v[76:79], v98 offset:17920\n"
      "v_fma_f32 v13, v13, s55, v105\n"
      "v_mfma_f32_16x16x32_bf16 v[52:55], a[228:231], v[88:91], v[52:55]\n"
      "v_exp_f32 v12, v12\n"
      "v_mfma_f32_16x16x32_bf16 v[56:59], a[232:235], v[88:91], v[56:59]\n"
      "s_waitcnt lgkmcnt(2)\n"
      "v_add_f32 v106, v106, v12\n"
      "v_add_u32 v104, 128, v104\n"
      "v_mfma_f32_16x16x32_bf16 v[56:59], a[248:251], v[92:95], v[56:59]\n"
      "v_exp_f32 v13, v13\n"
      "v_mfma_f32_16x16x32_bf16 v[60:63], a[252:255], v[92:95], v[60:63]\n"
      "v_add_f32 v106, v106, v13\n"
      "v_cvt_pk_bf16_f32 v10, v12, v13\n"
      "v_mfma_f32_16x16x32_bf16 v[60:63], a[236:239], v[88:91], v[60:63]\n"
      "v_fma_f32 v14, v14, s55, v105\n"
      "v_max3_f32 v108, v108, v33, v34\n"
      "v_mfma_f32_16x16x32_bf16 v[48:51], v[128:131], v[64:67], v[48:51]\n"
      "ds_read_b128 v[80:83], v98 offset:18944\n"
      "v_max3_f32 v108, v108, v35, v36\n"
      "v_mfma_f32_16x16x32_bf16 v[48:51], v[144:147], v[68:71], v[48:51]\n"
      "v_exp_f32 v14, v14\n"
      "v_mfma_f32_16x16x32_bf16 v[52:55], v[148:151], v[68:71], v[52:55]\n"
      "ds_read_b128 v[84:87], v98 offset:25344\n"
      "v_add_f32 v106, v106, v14\n"
      "v_mfma_f32_16x16x32_bf16 v[52:55], v[132:135], v[64:67], v[52:55]\n"
      "v_fma_f32 v15, v15, s55, v105\n"
      "v_max3_f32 v108, v108, v37, v38\n"
      "v_mfma_f32_16x16x32_bf16 v[56:59], v[136:139], v[64:67], v[56:59]\n"
      "s_waitcnt lgkmcnt(2)\n"
      "v_exp_f32 v15, v15\n"
      "v_mfma_f32_16x16x32_bf16 v[56:59], v[152:155], v[68:71], v[56:59]\n"
      "v_add_f32 v106, v106, v15\n"
      "v_cvt_pk_bf16_f32 v11, v14, v15\n"
      "v_mfma_f32_16x16x32_bf16 v[60:63], v[156:159], v[68:71], v[60:63]\n"
      "ds_write_b128 v99, v[8:11] offset:4096\n"
      "v_mfma_f32_16x16x32_bf16 v[60:63], v[140:143], v[64:67], v[60:63]\n"
      "v_add_u32 v96, s59, v97\n"
      "v_fma_f32 v16, v16, s55, v105\n"
      "v_mfma_f32_16x16x32_bf16 v[48:51], v[160:163], v[72:75], v[48:51]\n"
      "ds_read_b128 v[88:91], v98 offset:26368\n"
      "v_max3_f32 v108, v108, v39, v40\n"
      "v_mfma_f32_16x16x32_bf16 v[48:51], v[176:179], v[76:79], v[48:51]\n"
      "v_exp_f32 v16, v16\n"
      "v_mfma_f32_16x16x32_bf16 v[52:55], v[180:183], v[76:79], v[52:55]\n"
      "ds_read_b128 v[92:95], v98 offset:27392\n"
      "v_add_f32 v106, v106, v16\n"
      "v_mfma_f32_16x16x32_bf16 v[52:55], v[164:167], v[72:75], v[52:55]\n"
      "v_fma_f32 v17, v17, s55, v105\n"
      "v_max3_f32 v108, v108, v41, v42\n"
      "v_mfma_f32_16x16x32_bf16 v[56:59], v[168:171], v[72:75], v[56:59]\n"
      "s_waitcnt lgkmcnt(3)\n"
      "v_exp_f32 v17, v17\n"
      "v_mfma_f32_16x16x32_bf16 v[56:59], v[184:187], v[76:79], v[56:59]\n"
      "v_add_f32 v106, v106, v17\n"
      "v_cvt_pk_bf16_f32 v16, v16, v17\n"
      "v_mfma_f32_16x16x32_bf16 v[60:63], v[188:191], v[76:79], v[60:63]\n"
      "v_fma_f32 v18, v18, s55, v105\n"
      "v_max3_f32 v108, v108, v43, v44\n"
      "v_mfma_f32_16x16x32_bf16 v[60:63], v[172:175], v[72:75], v[60:63]\n"
      "v_exp_f32 v18, v18\n"
      "v_mfma_f32_16x16x32_bf16 v[48:51], v[192:195], v[80:83], v[48:51]\n"
      "ds_read_b128 v[64:67], v96\n"
      "v_add_f32 v106, v106, v18\n"
      "v_mfma_f32_16x16x32_bf16 v[48:51], v[208:211], v[84:87], v[48:51]\n"
      "v_fma_f32 v19, v19, s55, v105\n"
      "v_max3_f32 v108, v108, v45, v46\n"
      "v_mfma_f32_16x16x32_bf16 v[52:55], v[212:215], v[84:87], v[52:55]\n"
      "ds_read_b128 v[68:71], v96 offset:64\n"
      "v_fma_f32 v20, v20, s55, v105\n"
      "v_mfma_f32_16x16x32_bf16 v[52:55], v[196:199], v[80:83], v[52:55]\n"
      "v_exp_f32 v19, v19\n"
      "v_mfma_f32_16x16x32_bf16 v[56:59], v[200:203], v[80:83], v[56:59]\n"
      "s_waitcnt lgkmcnt(2)\n"
      "v_add_f32 v106, v106, v19\n"
      "v_cvt_pk_bf16_f32 v17, v18, v19\n"
      "v_mfma_f32_16x16x32_bf16 v[56:59], v[216:219], v[84:87], v[56:59]\n"
      "v_exp_f32 v20, v20\n"
      "v_mfma_f32_16x16x32_bf16 v[60:63], v[220:223], v[84:87], v[60:63]\n"
      "v_add_f32 v106, v106, v20\n"
      "v_max_f32 v108, v108, v47\n"
      "v_mfma_f32_16x16x32_bf16 v[60:63], v[204:207], v[80:83], v[60:63]\n"
      "ds_max_f32 v104, v108 offset:64\n"
      "v_mfma_f32_16x16x32_bf16 v[48:51], v[224:227], v[88:91], v[48:51]\n"
      "ds_read_b128 v[72:75], v96 offset:128\n"
      "v_fma_f32 v21, v21, s55, v105\n"
      "v_mfma_f32_16x16x32_bf16 v[48:51], v[240:243], v[92:95], v[48:51]\n"
      "v_exp_f32 v21, v21\n"
      "v_mfma_f32_16x16x32_bf16 v[52:55], v[244:247], v[92:95], v[52:55]\n"
      "ds_read_b128 v[76:79], v96 offset:192\n"
      "v_add_f32 v106, v106, v21\n"
      "v_mfma_f32_16x16x32_bf16 v[52:55], v[228:231], v[88:91], v[52:55]\n"
      "v_fma_f32 v22, v22, s55, v105\n"
      "v_cvt_pk_bf16_f32 v18, v20, v21\n"
      "v_mfma_f32_16x16x32_bf16 v[56:59], v[232:235], v[88:91], v[56:59]\n"
      "s_waitcnt lgkmcnt(3)\n"
      "v_exp_f32 v22, v22\n"
      "v_mfma_f32_16x16x32_bf16 v[56:59], v[248:251], v[92:95], v[56:59]\n"
      "v_add_f32 v106, v106, v22\n"
      "v_fma_f32 v23, v23, s55, v105\n"
      "v_mfma_f32_16x16x32_bf16 v[60:63], v[252:255], v[92:95], v[60:63]\n"
      "v_exp_f32 v23, v23\n"
      "v_mfma_f32_16x16x32_bf16 v[60:63], v[236:239], v[88:91], v[60:63]\n"
      "v_add_f32 v106, v106, v23\n"
      "v_cvt_pk_bf16_f32 v19, v22, v23\n"
      ".p2align 6\n"
      "1:\n"
      "v_mfma_f32_16x16x32_bf16 v[0:3], a[0:3], v[64:67], 0\n"
      "ds_read_b128 v[80:83], v96 offset:256\n"
      "v_cvt_u32_f32 v110, v106\n"
      "v_mfma_f32_16x16x32_bf16 v[0:3], a[4:7], v[68:71], v[0:3]\n"
      "ds_write_b128 v99, v[16:19] offset:5120\n"
      "ds_add_u32 v104, v110 offset:1536\n"
      "s_add_u32 s68, s68, 1\n"
      "v_mfma_f32_16x16x32_bf16 v[4:7], a[36:39], v[68:71], 0\n"
      "ds_read_b128 v[84:87], v96 offset:320\n"
      "v_mul_f32 v48, v107, v48\n"
      "v_mfma_f32_16x16x32_bf16 v[4:7], a[32:35], v[64:67], v[4:7]\n"
      "s_and_b32 s68, s68, 7\n"
      "v_mul_f32 v49, v107, v49\n"
      "v_cvt_pk_bf16_f32 v48, v48, v49\n"
      "v_mfma_f32_16x16x32_bf16 v[8:11], a[64:67], v[64:67], 0\n"
      "s_add_u32 s58, s68, 3\n"
      "v_mul_f32 v50, v107, v50\n"
      "v_mul_f32 v51, v107, v51\n"
      "v_mfma_f32_16x16x32_bf16 v[8:11], a[68:71], v[68:71], v[8:11]\n"
      "s_and_b32 s58, s58, 7\n"
      "v_cvt_pk_bf16_f32 v49, v50, v51\n"
      "v_mul_f32 v52, v107, v52\n"
      "v_mfma_f32_16x16x32_bf16 v[12:15], a[100:103], v[68:71], 0\n"
      "s_waitcnt vmcnt(8) lgkmcnt(1)\n"
      "s_barrier\n"
      "v_mul_f32 v53, v107, v53\n"
      "v_cvt_pk_bf16_f32 v50, v52, v53\n"
      "v_mfma_f32_16x16x32_bf16 v[12:15], a[96:99], v[64:67], v[12:15]\n"
      "s_mul_i32 s58, s58, 2112\n"
      "v_mul_f32 v54, v107, v54\n"
      "v_mul_f32 v55, v107, v55\n"
      "v_mfma_f32_16x16x32_bf16 v[16:19], a[128:131], v[64:67], 0\n"
      "v_cvt_pk_bf16_f32 v51, v54, v55\n"
      "v_mul_f32 v56, v107, v56\n"
      "v_mfma_f32_16x16x32_bf16 v[16:19], a[132:135], v[68:71], v[16:19]\n"
      "ds_write_b128 v100, v[48:51]\n"
      "v_mfma_f32_16x16x32_bf16 v[20:23], a[164:167], v[68:71], 0\n"
      "v_mul_f32 v57, v107, v57\n"
      "v_cvt_pk_bf16_f32 v52, v56, v57\n"
      "v_mfma_f32_16x16x32_bf16 v[20:23], a[160:163], v[64:67], v[20:23]\n"
      "ds_read_b32 v109, v104 offset:64\n"
      "v_mul_f32 v58, v107, v58\n"
      "v_mfma_f32_16x16x32_bf16 v[0:3], a[8:11], v[72:75], v[0:3]\n"
      "ds_read_b128 v[88:91], v96 offset:384\n"
      "v_mul_f32 v59, v107, v59\n"
      "v_mfma_f32_16x16x32_bf16 v[0:3], a[12:15], v[76:79], v[0:3]\n"
      "s_add_u32 m0, s53, s58\n"
      "v_cvt_pk_bf16_f32 v53, v58, v59\n"
      "v_mul_f32 v60, v107, v60\n"
      "v_mfma_f32_16x16x32_bf16 v[4:7], a[44:47], v[76:79], v[4:7]\n"
      "ds_read_b128 v[92:95], v96 offset:448\n"
      "v_mul_f32 v61, v107, v61\n"
      "v_mfma_f32_16x16x32_bf16 v[4:7], a[40:43], v[72:75], v[4:7]\n"
      "ds_read_b32 v112, v104 offset:1536\n"
      "v_cvt_pk_bf16_f32 v54, v60, v61\n"
      "v_mfma_f32_16x16x32_bf16 v[8:11], a[72:75], v[72:75], v[8:11]\n"
      "v_mul_f32 v62, v107, v62\n"
      "v_mul_f32 v63, v107, v63\n"
      "v_mfma_f32_16x16x32_bf16 v[8:11], a[76:79], v[76:79], v[8:11]\n"
      "s_waitcnt lgkmcnt(3)\n"
      "v_cvt_pk_bf16_f32 v55, v62, v63\n"
      "v_fma_f32 v105, v109, s56, v111\n"
      "v_mfma_f32_16x16x32_bf16 v[12:15], a[108:111], v[76:79], v[12:15]\n"
      "ds_write_b128 v100, v[52:55] offset:16\n"
      "v_mfma_f32_16x16x32_bf16 v[12:15], a[104:107], v[72:75], v[12:15]\n"
      "ds_read_b128 v[56:59], v101\n"
      "v_fma_f32 v24, v24, s55, v105\n"
      "v_mfma_f32_16x16x32_bf16 v[16:19], a[136:139], v[72:75], v[16:19]\n"
      "v_exp_f32 v24, v24\n"
      "v_mfma_f32_16x16x32_bf16 v[16:19], a[140:143], v[76:79], v[16:19]\n"
      "ds_read_b128 v[60:63], v101 offset:1152\n"
      "v_fma_f32 v25, v25, s55, v105\n"
      "v_mfma_f32_16x16x32_bf16 v[20:23], a[172:175], v[76:79], v[20:23]\n"
      "s_mul_i32 s59, s68, 2112\n"
      "v_exp_f32 v25, v25\n"
      "v_mfma_f32_16x16x32_bf16 v[20:23], a[168:171], v[72:75], v[20:23]\n"
      "s_waitcnt lgkmcnt(3)\n"
      "v_add_f32 v106, v24, v25\n"
      "v_cvt_pk_bf16_f32 v24, v24, v25\n"
      "v_mfma_f32_16x16x32_bf16 v[0:3], a[16:19], v[80:83], v[0:3]\n"
      "ds_read_b128 v[64:67], v98 offset:3072\n"
      "v_fma_f32 v26, v26, s55, v105\n"
      "v_mfma_f32_16x16x32_bf16 v[0:3], a[20:23], v[84:87], v[0:3]\n"
      "v_exp_f32 v26, v26\n"
      "v_mfma_f32_16x16x32_bf16 v[4:7], a[52:55], v[84:87], v[4:7]\n"
      "ds_read_b128 v[68:71], v98 offset:4096\n"
      "v_add_f32 v106, v106, v26\n"
      "v_mfma_f32_16x16x32_bf16 v[4:7], a[48:51], v[80:83], v[4:7]\n"
      "s_waitcnt lgkmcnt(2)\n"
      "v_fma_f32 v27, v27, s55, v105\n"
      "v_fma_f32 v28, v28, s55, v105\n"
      "v_mfma_f32_16x16x32_bf16 v[8:11], a[80:83], v[80:83], v[8:11]\n"
      "buffer_store_dwordx4 v[56:59], v103, s[48:51], s60 offen sc1\n"
      "v_cvt_f32_u32 v112, v112\n"
      "v_mfma_f32_16x16x32_bf16 v[8:11], a[84:87], v[84:87], v[8:11]\n"
      "s_add_u32 s60, s60, 8192\n"
      "v_exp_f32 v27, v27\n"
      "v_mfma_f32_16x16x32_bf16 v[12:15], a[116:119], v[84:87], v[12:15]\n"
      "v_add_f32 v106, v106, v27\n"
      "v_cvt_pk_bf16_f32 v25, v26, v27\n"
      "v_mfma_f32_16x16x32_bf16 v[12:15], a[112:115], v[80:83], v[12:15]\n"
      "v_exp_f32 v28, v28\n"
      "v_mfma_f32_16x16x32_bf16 v[16:19], a[144:147], v[80:83], v[16:19]\n"
      "v_add_f32 v106, v106, v28\n"
      "v_fma_f32 v29, v29, s55, v105\n"
      "v_mfma_f32_16x16x32_bf16 v[16:19], a[148:151], v[84:87], v[16:19]\n"
      "buffer_store_dwordx4 v[60:63], v103, s[48:51], s61 offen sc1\n"
      "v_fma_f32 v30, v30, s55, v105\n"
      "v_mfma_f32_16x16x32_bf16 v[20:23], a[180:183], v[84:87], v[20:23]\n"
      "s_add_u32 s61, s61, 8192\n"
      "v_rcp_f32 v107, v112\n"
      "v_mfma_f32_16x16x32_bf16 v[20:23], a[176:179], v[80:83], v[20:23]\n"
      "v_exp_f32 v29, v29\n"
      "v_mfma_f32_16x16x32_bf16 v[0:3], a[24:27], v[88:91], v[0:3]\n"
      "ds_read_b128 v[72:75], v98 offset:5120\n"
      "v_add_f32 v106, v106, v29\n"
      "v_mfma_f32_16x16x32_bf16 v[0:3], a[28:31], v[92:95], v[0:3]\n"
      "v_fma_f32 v31, v31, s55, v105\n"
      "v_cvt_pk_bf16_f32 v26, v28, v29\n"
      "v_mfma_f32_16x16x32_bf16 v[4:7], a[60:63], v[92:95], v[4:7]\n"
      "ds_read_b128 v[76:79], v98 offset:11520\n"
      "v_fma_f32 v32, v32, s55, v105\n"
      "v_mfma_f32_16x16x32_bf16 v[4:7], a[56:59], v[88:91], v[4:7]\n"
      "v_exp_f32 v30, v30\n"
      "v_mfma_f32_16x16x32_bf16 v[8:11], a[88:91], v[88:91], v[8:11]\n"
      "v_add_f32 v106, v106, v30\n"
      "v_fma_f32 v33, v33, s55, v105\n"
      "v_mfma_f32_16x16x32_bf16 v[8:11], a[92:95], v[92:95], v[8:11]\n"
      "v_exp_f32 v31, v31\n"
      "v_mfma_f32_16x16x32_bf16 v[12:15], a[124:127], v[92:95], v[12:15]\n"
      "v_add_f32 v106, v106, v31\n"
      "v_cvt_pk_bf16_f32 v27, v30, v31\n"
      "v_mfma_f32_16x16x32_bf16 v[12:15], a[120:123], v[88:91], v[12:15]\n"
      "ds_write_b128 v99, v[24:27]\n"
      "v_mfma_f32_16x16x32_bf16 v[16:19], a[152:155], v[88:91], v[16:19]\n"
      "s_waitcnt lgkmcnt(3)\n"
      "v_exp_f32 v32, v32\n"
      "v_mfma_f32_16x16x32_bf16 v[16:19], a[156:159], v[92:95], v[16:19]\n"
      "v_add_f32 v106, v106, v32\n"
      "v_fma_f32 v34, v34, s55, v105\n"
      "v_mfma_f32_16x16x32_bf16 v[20:23], a[188:191], v[92:95], v[20:23]\n"
      "v_exp_f32 v33, v33\n"
      "v_mfma_f32_16x16x32_bf16 v[20:23], a[184:187], v[88:91], v[20:23]\n"
      "v_add_f32 v106, v106, v33\n"
      "v_cvt_pk_bf16_f32 v32, v32, v33\n"
      "v_mfma_f32_16x16x32_bf16 v[48:51], a[192:195], v[64:67], 0\n"
      "ds_read_b128 v[80:83], v98 offset:12544\n"
      "v_max3_f32 v108, v0, v1, v2\n"
      "v_mfma_f32_16x16x32_bf16 v[48:51], a[208:211], v[68:71], v[48:51]\n"
      "buffer_load_dwordx4 v102, s[36:39], s57 offen nt lds\n"
      "v_mfma_f32_16x16x32_bf16 v[52:55], a[212:215], v[68:71], 0\n"
      "ds_read_b128 v[84:87], v98 offset:13568\n"
      "v_fma_f32 v35, v35, s55, v105\n"
      "v_mfma_f32_16x16x32_bf16 v[52:55], a[196:199], v[64:67], v[52:55]\n"
      "s_add_u32 m0, m0, 544\n"
      "v_exp_f32 v34, v34\n"
      "v_mfma_f32_16x16x32_bf16 v[56:59], a[200:203], v[64:67], 0\n"
      "s_waitcnt lgkmcnt(3)\n"
      "v_add_f32 v106, v106, v34\n"
      "v_max3_f32 v108, v108, v3, v4\n"
      "v_mfma_f32_16x16x32_bf16 v[56:59], a[216:219], v[68:71], v[56:59]\n"
      "buffer_load_dwordx4 v102, s[36:39], s57 offen offset:512 nt lds\n"
      "v_mfma_f32_16x16x32_bf16 v[60:63], a[220:223], v[68:71], 0\n"
      "s_add_u32 s57, s57, 8192\n"
      "v_exp_f32 v35, v35\n"
      "v_mfma_f32_16x16x32_bf16 v[60:63], a[204:207], v[64:67], v[60:63]\n"
      "v_add_f32 v106, v106, v35\n"
      "v_cvt_pk_bf16_f32 v33, v34, v35\n"
      "v_mfma_f32_16x16x32_bf16 v[48:51], a[224:227], v[72:75], v[48:51]\n"
      "ds_read_b128 v[88:91], v98 offset:19968\n"
      "v_fma_f32 v36, v36, s55, v105\n"
      "v_mfma_f32_16x16x32_bf16 v[48:51], a[240:243], v[76:79], v[48:51]\n"
      "v_max3_f32 v108, v108, v5, v6\n"
      "v_max3_f32 v108, v108, v7, v8\n"
      "v_mfma_f32_16x16x32_bf16 v[52:55], a[244:247], v[76:79], v[52:55]\n"
      "ds_read_b128 v[92:95], v98 offset:20992\n"
      "v_fma_f32 v37, v37, s55, v105\n"
      "v_mfma_f32_16x16x32_bf16 v[52:55], a[228:231], v[72:75], v[52:55]\n"
      "v_exp_f32 v36, v36\n"
      "v_mfma_f32_16x16x32_bf16 v[56:59], a[232:235], v[72:75], v[56:59]\n"
      "s_waitcnt lgkmcnt(2)\n"
      "v_add_f32 v106, v106, v36\n"
      "v_mfma_f32_16x16x32_bf16 v[56:59], a[248:251], v[76:79], v[56:59]\n"
      "v_exp_f32 v37, v37\n"
      "v_mfma_f32_16x16x32_bf16 v[60:63], a[252:255], v[76:79], v[60:63]\n"
      "v_add_f32 v106, v106, v37\n"
      "v_cvt_pk_bf16_f32 v34, v36, v37\n"
      "v_mfma_f32_16x16x32_bf16 v[60:63], a[236:239], v[72:75], v[60:63]\n"
      "v_fma_f32 v38, v38, s55, v105\n"
      "v_max3_f32 v108, v108, v9, v10\n"
      "v_mfma_f32_16x16x32_bf16 v[48:51], v[128:131], v[80:83], v[48:51]\n"
      "ds_read_b128 v[64:67], v98 offset:22016\n"
      "v_max3_f32 v108, v108, v11, v12\n"
      "v_mfma_f32_16x16x32_bf16 v[48:51], v[144:147], v[84:87], v[48:51]\n"
      "v_exp_f32 v38, v38\n"
      "v_mfma_f32_16x16x32_bf16 v[52:55], v[148:151], v[84:87], v[52:55]\n"
      "ds_read_b128 v[68:71], v98 offset:28416\n"
      "v_add_f32 v106, v106, v38\n"
      "v_mfma_f32_16x16x32_bf16 v[52:55], v[132:135], v[80:83], v[52:55]\n"
      "v_fma_f32 v39, v39, s55, v105\n"
      "v_max3_f32 v108, v108, v13, v14\n"
      "v_mfma_f32_16x16x32_bf16 v[56:59], v[136:139], v[80:83], v[56:59]\n"
      "s_waitcnt lgkmcnt(2)\n"
      "v_exp_f32 v39, v39\n"
      "v_mfma_f32_16x16x32_bf16 v[56:59], v[152:155], v[84:87], v[56:59]\n"
      "v_add_f32 v106, v106, v39\n"
      "v_cvt_pk_bf16_f32 v35, v38, v39\n"
      "v_mfma_f32_16x16x32_bf16 v[60:63], v[156:159], v[84:87], v[60:63]\n"
      "ds_write_b128 v99, v[32:35] offset:1024\n"
      "v_mfma_f32_16x16x32_bf16 v[60:63], v[140:143], v[80:83], v[60:63]\n"
      "v_add_u32 v96, s59, v97\n"
      "v_fma_f32 v40, v40, s55, v105\n"
      "v_mfma_f32_16x16x32_bf16 v[48:51], v[160:163], v[88:91], v[48:51]\n"
      "ds_read_b128 v[72:75], v98 offset:29440\n"
      "v_max3_f32 v108, v108, v15, v16\n"
      "v_mfma_f32_16x16x32_bf16 v[48:51], v[176:179], v[92:95], v[48:51]\n"
      "v_exp_f32 v40, v40\n"
      "v_mfma_f32_16x16x32_bf16 v[52:55], v[180:183], v[92:95], v[52:55]\n"
      "ds_read_b128 v[76:79], v98 offset:30464\n"
      "v_add_f32 v106, v106, v40\n"
      "v_mfma_f32_16x16x32_bf16 v[52:55], v[164:167], v[88:91], v[52:55]\n"
      "v_fma_f32 v41, v41, s55, v105\n"
      "v_max3_f32 v108, v108, v17, v18\n"
      "v_mfma_f32_16x16x32_bf16 v[56:59], v[168:171], v[88:91], v[56:59]\n"
      "s_waitcnt lgkmcnt(3)\n"
      "v_exp_f32 v41, v41\n"
      "v_mfma_f32_16x16x32_bf16 v[56:59], v[184:187], v[92:95], v[56:59]\n"
      "v_add_f32 v106, v106, v41\n"
      "v_cvt_pk_bf16_f32 v40, v40, v41\n"
      "v_mfma_f32_16x16x32_bf16 v[60:63], v[188:191], v[92:95], v[60:63]\n"
      "v_fma_f32 v42, v42, s55, v105\n"
      "v_max3_f32 v108, v108, v19, v20\n"
      "v_mfma_f32_16x16x32_bf16 v[60:63], v[172:175], v[88:91], v[60:63]\n"
      "v_exp_f32 v42, v42\n"
      "v_mfma_f32_16x16x32_bf16 v[48:51], v[192:195], v[64:67], v[48:51]\n"
      "ds_read_b128 v[80:83], v96\n"
      "v_add_f32 v106, v106, v42\n"
      "v_mfma_f32_16x16x32_bf16 v[48:51], v[208:211], v[68:71], v[48:51]\n"
      "v_fma_f32 v43, v43, s55, v105\n"
      "v_max3_f32 v108, v108, v21, v22\n"
      "v_mfma_f32_16x16x32_bf16 v[52:55], v[212:215], v[68:71], v[52:55]\n"
      "ds_read_b128 v[84:87], v96 offset:64\n"
      "v_fma_f32 v44, v44, s55, v105\n"
      "v_mfma_f32_16x16x32_bf16 v[52:55], v[196:199], v[64:67], v[52:55]\n"
      "v_exp_f32 v43, v43\n"
      "v_mfma_f32_16x16x32_bf16 v[56:59], v[200:203], v[64:67], v[56:59]\n"
      "s_waitcnt lgkmcnt(2)\n"
      "v_add_f32 v106, v106, v43\n"
      "v_cvt_pk_bf16_f32 v41, v42, v43\n"
      "v_mfma_f32_16x16x32_bf16 v[56:59], v[216:219], v[68:71], v[56:59]\n"
      "v_exp_f32 v44, v44\n"
      "v_mfma_f32_16x16x32_bf16 v[60:63], v[220:223], v[68:71], v[60:63]\n"
      "v_add_f32 v106, v106, v44\n"
      "v_max_f32 v108, v108, v23\n"
      "v_mfma_f32_16x16x32_bf16 v[60:63], v[204:207], v[64:67], v[60:63]\n"
      "ds_max_f32 v104, v108 offset:128\n"
      "v_mfma_f32_16x16x32_bf16 v[48:51], v[224:227], v[72:75], v[48:51]\n"
      "ds_read_b128 v[88:91], v96 offset:128\n"
      "v_fma_f32 v45, v45, s55, v105\n"
      "v_mfma_f32_16x16x32_bf16 v[48:51], v[240:243], v[76:79], v[48:51]\n"
      "v_exp_f32 v45, v45\n"
      "v_mfma_f32_16x16x32_bf16 v[52:55], v[244:247], v[76:79], v[52:55]\n"
      "ds_read_b128 v[92:95], v96 offset:192\n"
      "v_add_f32 v106, v106, v45\n"
      "v_mfma_f32_16x16x32_bf16 v[52:55], v[228:231], v[72:75], v[52:55]\n"
      "v_fma_f32 v46, v46, s55, v105\n"
      "v_cvt_pk_bf16_f32 v42, v44, v45\n"
      "v_mfma_f32_16x16x32_bf16 v[56:59], v[232:235], v[72:75], v[56:59]\n"
      "s_waitcnt lgkmcnt(3)\n"
      "v_exp_f32 v46, v46\n"
      "v_mfma_f32_16x16x32_bf16 v[56:59], v[248:251], v[76:79], v[56:59]\n"
      "v_add_f32 v106, v106, v46\n"
      "v_fma_f32 v47, v47, s55, v105\n"
      "v_mfma_f32_16x16x32_bf16 v[60:63], v[252:255], v[76:79], v[60:63]\n"
      "v_exp_f32 v47, v47\n"
      "v_mfma_f32_16x16x32_bf16 v[60:63], v[236:239], v[72:75], v[60:63]\n"
      "v_add_f32 v106, v106, v47\n"
      "v_cvt_pk_bf16_f32 v43, v46, v47\n"
      "v_mfma_f32_16x16x32_bf16 v[24:27], a[0:3], v[80:83], 0\n"
      "ds_read_b128 v[64:67], v96 offset:256\n"
      "v_cvt_u32_f32 v110, v106\n"
      "v_mfma_f32_16x16x32_bf16 v[24:27], a[4:7], v[84:87], v[24:27]\n"
      "ds_write_b128 v99, v[40:43] offset:2048\n"
      "ds_add_u32 v104, v110 offset:1600\n"
      "s_add_u32 s68, s68, 1\n"
      "v_mfma_f32_16x16x32_bf16 v[28:31], a[36:39], v[84:87], 0\n"
      "ds_read_b128 v[68:71], v96 offset:320\n"
      "v_mul_f32 v48, v107, v48\n"
      "v_mfma_f32_16x16x32_bf16 v[28:31], a[32:35], v[80:83], v[28:31]\n"
      "s_and_b32 s68, s68, 7\n"
      "v_mul_f32 v49, v107, v49\n"
      "v_cvt_pk_bf16_f32 v48, v48, v49\n"
      "v_mfma_f32_16x16x32_bf16 v[32:35], a[64:67], v[80:83], 0\n"
      "s_add_u32 s58, s68, 3\n"
      "v_mul_f32 v50, v107, v50\n"
      "v_mul_f32 v51, v107, v51\n"
      "v_mfma_f32_16x16x32_bf16 v[32:35], a[68:71], v[84:87], v[32:35]\n"
      "s_and_b32 s58, s58, 7\n"
      "v_cvt_pk_bf16_f32 v49, v50, v51\n"
      "v_mul_f32 v52, v107, v52\n"
      "v_mfma_f32_16x16x32_bf16 v[36:39], a[100:103], v[84:87], 0\n"
      "s_waitcnt vmcnt(8) lgkmcnt(1)\n"
      "s_barrier\n"
      "v_mul_f32 v53, v107, v53\n"
      "v_cvt_pk_bf16_f32 v50, v52, v53\n"
      "v_mfma_f32_16x16x32_bf16 v[36:39], a[96:99], v[80:83], v[36:39]\n"
      "s_mul_i32 s58, s58, 2112\n"
      "v_mul_f32 v54, v107, v54\n"
      "v_mul_f32 v55, v107, v55\n"
      "v_mfma_f32_16x16x32_bf16 v[40:43], a[128:131], v[80:83], 0\n"
      "s_sub_u32 s62, s62, 1\n"
      "v_cvt_pk_bf16_f32 v51, v54, v55\n"
      "v_mul_f32 v56, v107, v56\n"
      "v_mfma_f32_16x16x32_bf16 v[40:43], a[132:135], v[84:87], v[40:43]\n"
      "ds_write_b128 v100, v[48:51]\n"
      "v_mfma_f32_16x16x32_bf16 v[44:47], a[164:167], v[84:87], 0\n"
      "v_mul_f32 v57, v107, v57\n"
      "v_cvt_pk_bf16_f32 v52, v56, v57\n"
      "v_mfma_f32_16x16x32_bf16 v[44:47], a[160:163], v[80:83], v[44:47]\n"
      "ds_read_b32 v109, v104 offset:128\n"
      "v_mul_f32 v58, v107, v58\n"
      "v_mfma_f32_16x16x32_bf16 v[24:27], a[8:11], v[88:91], v[24:27]\n"
      "ds_read_b128 v[72:75], v96 offset:384\n"
      "v_mul_f32 v59, v107, v59\n"
      "v_mfma_f32_16x16x32_bf16 v[24:27], a[12:15], v[92:95], v[24:27]\n"
      "s_add_u32 m0, s53, s58\n"
      "v_cvt_pk_bf16_f32 v53, v58, v59\n"
      "v_mul_f32 v60, v107, v60\n"
      "v_mfma_f32_16x16x32_bf16 v[28:31], a[44:47], v[92:95], v[28:31]\n"
      "ds_read_b128 v[76:79], v96 offset:448\n"
      "v_mul_f32 v61, v107, v61\n"
      "v_mfma_f32_16x16x32_bf16 v[28:31], a[40:43], v[88:91], v[28:31]\n"
      "ds_read_b32 v112, v104 offset:1600\n"
      "v_cvt_pk_bf16_f32 v54, v60, v61\n"
      "v_mfma_f32_16x16x32_bf16 v[32:35], a[72:75], v[88:91], v[32:35]\n"
      "v_mul_f32 v62, v107, v62\n"
      "v_mul_f32 v63, v107, v63\n"
      "v_mfma_f32_16x16x32_bf16 v[32:35], a[76:79], v[92:95], v[32:35]\n"
      "s_waitcnt lgkmcnt(3)\n"
      "v_cvt_pk_bf16_f32 v55, v62, v63\n"
      "v_fma_f32 v105, v109, s56, v111\n"
      "v_mfma_f32_16x16x32_bf16 v[36:39], a[108:111], v[92:95], v[36:39]\n"
      "ds_write_b128 v100, v[52:55] offset:16\n"
      "v_mfma_f32_16x16x32_bf16 v[36:39], a[104:107], v[88:91], v[36:39]\n"
      "ds_read_b128 v[56:59], v101\n"
      "v_fma_f32 v0, v0, s55, v105\n"
      "v_mfma_f32_16x16x32_bf16 v[40:43], a[136:139], v[88:91], v[40:43]\n"
      "v_exp_f32 v0, v0\n"
      "v_mfma_f32_16x16x32_bf16 v[40:43], a[140:143], v[92:95], v[40:43]\n"
      "ds_read_b128 v[60:63], v101 offset:1152\n"
      "v_fma_f32 v1, v1, s55, v105\n"
      "v_mfma_f32_16x16x32_bf16 v[44:47], a[172:175], v[92:95], v[44:47]\n"
      "s_mul_i32 s59, s68, 2112\n"
      "v_exp_f32 v1, v1\n"
      "v_mfma_f32_16x16x32_bf16 v[44:47], a[168:171], v[88:91], v[44:47]\n"
      "s_waitcnt lgkmcnt(3)\n"
      "v_add_f32 v106, v0, v1\n"
      "v_cvt_pk_bf16_f32 v0, v0, v1\n"
      "v_mfma_f32_16x16x32_bf16 v[24:27], a[16:19], v[64:67], v[24:27]\n"
      "ds_read_b128 v[80:83], v98\n"
      "v_fma_f32 v2, v2, s55, v105\n"
      "v_mfma_f32_16x16x32_bf16 v[24:27], a[20:23], v[68:71], v[24:27]\n"
      "v_exp_f32 v2, v2\n"
      "v_mfma_f32_16x16x32_bf16 v[28:31], a[52:55], v[68:71], v[28:31]\n"
      "ds_read_b128 v[84:87], v98 offset:1024\n"
      "v_add_f32 v106, v106, v2\n"
      "v_mfma_f32_16x16x32_bf16 v[28:31], a[48:51], v[64:67], v[28:31]\n"
      "s_waitcnt lgkmcnt(2)\n"
      "v_fma_f32 v3, v3, s55, v105\n"
      "v_fma_f32 v4, v4, s55, v105\n"
      "v_mfma_f32_16x16x32_bf16 v[32:35], a[80:83], v[64:67], v[32:35]\n"
      "buffer_store_dwordx4 v[56:59], v103, s[48:51], s60 offen sc1\n"
      "v_cvt_f32_u32 v112, v112\n"
      "v_mfma_f32_16x16x32_bf16 v[32:35], a[84:87], v[68:71], v[32:35]\n"
      "s_add_u32 s60, s60, 8192\n"
      "v_exp_f32 v3, v3\n"
      "v_mfma_f32_16x16x32_bf16 v[36:39], a[116:119], v[68:71], v[36:39]\n"
      "v_add_f32 v106, v106, v3\n"
      "v_cvt_pk_bf16_f32 v1, v2, v3\n"
      "v_mfma_f32_16x16x32_bf16 v[36:39], a[112:115], v[64:67], v[36:39]\n"
      "v_exp_f32 v4, v4\n"
      "v_mfma_f32_16x16x32_bf16 v[40:43], a[144:147], v[64:67], v[40:43]\n"
      "v_add_f32 v106, v106, v4\n"
      "v_fma_f32 v5, v5, s55, v105\n"
      "v_mfma_f32_16x16x32_bf16 v[40:43], a[148:151], v[68:71], v[40:43]\n"
      "buffer_store_dwordx4 v[60:63], v103, s[48:51], s61 offen sc1\n"
      "v_fma_f32 v6, v6, s55, v105\n"
      "v_mfma_f32_16x16x32_bf16 v[44:47], a[180:183], v[68:71], v[44:47]\n"
      "s_add_u32 s61, s61, 8192\n"
      "v_rcp_f32 v107, v112\n"
      "v_mfma_f32_16x16x32_bf16 v[44:47], a[176:179], v[64:67], v[44:47]\n"
      "v_exp_f32 v5, v5\n"
      "v_mfma_f32_16x16x32_bf16 v[24:27], a[24:27], v[72:75], v[24:27]\n"
      "ds_read_b128 v[88:91], v98 offset:2048\n"
      "v_add_f32 v106, v106, v5\n"
      "v_mfma_f32_16x16x32_bf16 v[24:27], a[28:31], v[76:79], v[24:27]\n"
      "v_fma_f32 v7, v7, s55, v105\n"
      "v_cvt_pk_bf16_f32 v2, v4, v5\n"
      "v_mfma_f32_16x16x32_bf16 v[28:31], a[60:63], v[76:79], v[28:31]\n"
      "ds_read_b128 v[92:95], v98 offset:8448\n"
      "v_fma_f32 v8, v8, s55, v105\n"
      "v_mfma_f32_16x16x32_bf16 v[28:31], a[56:59], v[72:75], v[28:31]\n"
      "v_exp_f32 v6, v6\n"
      "v_mfma_f32_16x16x32_bf16 v[32:35], a[88:91], v[72:75], v[32:35]\n"
      "v_add_f32 v106, v106, v6\n"
      "v_fma_f32 v9, v9, s55, v105\n"
      "v_mfma_f32_16x16x32_bf16 v[32:35], a[92:95], v[76:79], v[32:35]\n"
      "v_exp_f32 v7, v7\n"
      "v_mfma_f32_16x16x32_bf16 v[36:39], a[124:127], v[76:79], v[36:39]\n"
      "v_add_f32 v106, v106, v7\n"
      "v_cvt_pk_bf16_f32 v3, v6, v7\n"
      "v_mfma_f32_16x16x32_bf16 v[36:39], a[120:123], v[72:75], v[36:39]\n"
      "ds_write_b128 v99, v[0:3] offset:3072\n"
      "v_mfma_f32_16x16x32_bf16 v[40:43], a[152:155], v[72:75], v[40:43]\n"
      "s_waitcnt lgkmcnt(3)\n"
      "v_exp_f32 v8, v8\n"
      "v_mfma_f32_16x16x32_bf16 v[40:43], a[156:159], v[76:79], v[40:43]\n"
      "v_add_f32 v106, v106, v8\n"
      "v_fma_f32 v10, v10, s55, v105\n"
      "v_mfma_f32_16x16x32_bf16 v[44:47], a[188:191], v[76:79], v[44:47]\n"
      "v_exp_f32 v9, v9\n"
      "v_mfma_f32_16x16x32_bf16 v[44:47], a[184:187], v[72:75], v[44:47]\n"
      "v_add_f32 v106, v106, v9\n"
      "v_cvt_pk_bf16_f32 v8, v8, v9\n"
      "v_mfma_f32_16x16x32_bf16 v[48:51], a[192:195], v[80:83], 0\n"
      "ds_read_b128 v[64:67], v98 offset:9472\n"
      "v_max3_f32 v108, v24, v25, v26\n"
      "v_mfma_f32_16x16x32_bf16 v[48:51], a[208:211], v[84:87], v[48:51]\n"
      "buffer_load_dwordx4 v102, s[36:39], s57 offen nt lds\n"
      "v_mfma_f32_16x16x32_bf16 v[52:55], a[212:215], v[84:87], 0\n"
      "ds_read_b128 v[68:71], v98 offset:10496\n"
      "v_fma_f32 v11, v11, s55, v105\n"
      "v_mfma_f32_16x16x32_bf16 v[52:55], a[196:199], v[80:83], v[52:55]\n"
      "s_add_u32 m0, m0, 544\n"
      "v_exp_f32 v10, v10\n"
      "v_mfma_f32_16x16x32_bf16 v[56:59], a[200:203], v[80:83], 0\n"
      "s_waitcnt lgkmcnt(3)\n"
      "v_add_f32 v106, v106, v10\n"
      "v_max3_f32 v108, v108, v27, v28\n"
      "v_mfma_f32_16x16x32_bf16 v[56:59], a[216:219], v[84:87], v[56:59]\n"
      "buffer_load_dwordx4 v102, s[36:39], s57 offen offset:512 nt lds\n"
      "v_mfma_f32_16x16x32_bf16 v[60:63], a[220:223], v[84:87], 0\n"
      "s_add_u32 s57, s57, 8192\n"
      "v_exp_f32 v11, v11\n"
      "v_mfma_f32_16x16x32_bf16 v[60:63], a[204:207], v[80:83], v[60:63]\n"
      "v_add_f32 v106, v106, v11\n"
      "v_cvt_pk_bf16_f32 v9, v10, v11\n"
      "v_mfma_f32_16x16x32_bf16 v[48:51], a[224:227], v[88:91], v[48:51]\n"
      "ds_read_b128 v[72:75], v98 offset:16896\n"
      "v_fma_f32 v12, v12, s55, v105\n"
      "v_mfma_f32_16x16x32_bf16 v[48:51], a[240:243], v[92:95], v[48:51]\n"
      "v_max3_f32 v108, v108, v29, v30\n"
      "v_max3_f32 v108, v108, v31, v32\n"
      "v_mfma_f32_16x16x32_bf16 v[52:55], a[244:247], v[92:95], v[52:55]\n"
      "ds_read_b128 v[76:79], v98 offset:17920\n"
      "v_fma_f32 v13, v13, s55, v105\n"
      "v_mfma_f32_16x16x32_bf16 v[52:55], a[228:231], v[88:91], v[52:55]\n"
      "v_exp_f32 v12, v12\n"
      "v_mfma_f32_16x16x32_bf16 v[56:59], a[232:235], v[88:91], v[56:59]\n"
      "s_waitcnt lgkmcnt(2)\n"
      "v_add_f32 v106, v106, v12\n"
      "v_add_u32 v104, 128, v104\n"
      "v_mfma_f32_16x16x32_bf16 v[56:59], a[248:251], v[92:95], v[56:59]\n"
      "v_exp_f32 v13, v13\n"
      "v_mfma_f32_16x16x32_bf16 v[60:63], a[252:255], v[92:95], v[60:63]\n"
      "v_add_f32 v106, v106, v13\n"
      "v_cvt_pk_bf16_f32 v10, v12, v13\n"
      "v_mfma_f32_16x16x32_bf16 v[60:63], a[236:239], v[88:91], v[60:63]\n"
      "v_fma_f32 v14, v14, s55, v105\n"
      "v_max3_f32 v108, v108, v33, v34\n"
      "v_mfma_f32_16x16x32_bf16 v[48:51], v[128:131], v[64:67], v[48:51]\n"
      "ds_read_b128 v[80:83], v98 offset:18944\n"
      "v_max3_f32 v108, v108, v35, v36\n"
      "v_mfma_f32_16x16x32_bf16 v[48:51], v[144:147], v[68:71], v[48:51]\n"
      "v_exp_f32 v14, v14\n"
      "v_mfma_f32_16x16x32_bf16 v[52:55], v[148:151], v[68:71], v[52:55]\n"
      "ds_read_b128 v[84:87], v98 offset:25344\n"
      "v_add_f32 v106, v106, v14\n"
      "v_mfma_f32_16x16x32_bf16 v[52:55], v[132:135], v[64:67], v[52:55]\n"
      "v_fma_f32 v15, v15, s55, v105\n"
      "v_max3_f32 v108, v108, v37, v38\n"
      "v_mfma_f32_16x16x32_bf16 v[56:59], v[136:139], v[64:67], v[56:59]\n"
      "s_waitcnt lgkmcnt(2)\n"
      "v_exp_f32 v15, v15\n"
      "v_mfma_f32_16x16x32_bf16 v[56:59], v[152:155], v[68:71], v[56:59]\n"
      "v_add_f32 v106, v106, v15\n"
      "v_cvt_pk_bf16_f32 v11, v14, v15\n"
      "v_mfma_f32_16x16x32_bf16 v[60:63], v[156:159], v[68:71], v[60:63]\n"
      "ds_write_b128 v99, v[8:11] offset:4096\n"
      "v_mfma_f32_16x16x32_bf16 v[60:63], v[140:143], v[64:67], v[60:63]\n"
      "v_add_u32 v96, s59, v97\n"
      "v_fma_f32 v16, v16, s55, v105\n"
      "v_mfma_f32_16x16x32_bf16 v[48:51], v[160:163], v[72:75], v[48:51]\n"
      "ds_read_b128 v[88:91], v98 offset:26368\n"
      "v_max3_f32 v108, v108, v39, v40\n"
      "v_mfma_f32_16x16x32_bf16 v[48:51], v[176:179], v[76:79], v[48:51]\n"
      "v_exp_f32 v16, v16\n"
      "v_mfma_f32_16x16x32_bf16 v[52:55], v[180:183], v[76:79], v[52:55]\n"
      "ds_read_b128 v[92:95], v98 offset:27392\n"
      "v_add_f32 v106, v106, v16\n"
      "v_mfma_f32_16x16x32_bf16 v[52:55], v[164:167], v[72:75], v[52:55]\n"
      "v_fma_f32 v17, v17, s55, v105\n"
      "v_max3_f32 v108, v108, v41, v42\n"
      "v_mfma_f32_16x16x32_bf16 v[56:59], v[168:171], v[72:75], v[56:59]\n"
      "s_waitcnt lgkmcnt(3)\n"
      "v_exp_f32 v17, v17\n"
      "v_mfma_f32_16x16x32_bf16 v[56:59], v[184:187], v[76:79], v[56:59]\n"
      "v_add_f32 v106, v106, v17\n"
      "v_cvt_pk_bf16_f32 v16, v16, v17\n"
      "v_mfma_f32_16x16x32_bf16 v[60:63], v[188:191], v[76:79], v[60:63]\n"
      "v_fma_f32 v18, v18, s55, v105\n"
      "v_max3_f32 v108, v108, v43, v44\n"
      "v_mfma_f32_16x16x32_bf16 v[60:63], v[172:175], v[72:75], v[60:63]\n"
      "v_exp_f32 v18, v18\n"
      "v_mfma_f32_16x16x32_bf16 v[48:51], v[192:195], v[80:83], v[48:51]\n"
      "ds_read_b128 v[64:67], v96\n"
      "v_add_f32 v106, v106, v18\n"
      "v_mfma_f32_16x16x32_bf16 v[48:51], v[208:211], v[84:87], v[48:51]\n"
      "v_fma_f32 v19, v19, s55, v105\n"
      "v_max3_f32 v108, v108, v45, v46\n"
      "v_mfma_f32_16x16x32_bf16 v[52:55], v[212:215], v[84:87], v[52:55]\n"
      "ds_read_b128 v[68:71], v96 offset:64\n"
      "v_fma_f32 v20, v20, s55, v105\n"
      "v_mfma_f32_16x16x32_bf16 v[52:55], v[196:199], v[80:83], v[52:55]\n"
      "v_exp_f32 v19, v19\n"
      "v_mfma_f32_16x16x32_bf16 v[56:59], v[200:203], v[80:83], v[56:59]\n"
      "s_waitcnt lgkmcnt(2)\n"
      "v_add_f32 v106, v106, v19\n"
      "v_cvt_pk_bf16_f32 v17, v18, v19\n"
      "v_mfma_f32_16x16x32_bf16 v[56:59], v[216:219], v[84:87], v[56:59]\n"
      "v_exp_f32 v20, v20\n"
      "v_mfma_f32_16x16x32_bf16 v[60:63], v[220:223], v[84:87], v[60:63]\n"
      "v_add_f32 v106, v106, v20\n"
      "v_max_f32 v108, v108, v47\n"
      "v_mfma_f32_16x16x32_bf16 v[60:63], v[204:207], v[80:83], v[60:63]\n"
      "ds_max_f32 v104, v108 offset:64\n"
      "v_mfma_f32_16x16x32_bf16 v[48:51], v[224:227], v[88:91], v[48:51]\n"
      "ds_read_b128 v[72:75], v96 offset:128\n"
      "v_fma_f32 v21, v21, s55, v105\n"
      "v_mfma_f32_16x16x32_bf16 v[48:51], v[240:243], v[92:95], v[48:51]\n"
      "v_exp_f32 v21, v21\n"
      "v_mfma_f32_16x16x32_bf16 v[52:55], v[244:247], v[92:95], v[52:55]\n"
      "ds_read_b128 v[76:79], v96 offset:192\n"
      "v_add_f32 v106, v106, v21\n"
      "v_mfma_f32_16x16x32_bf16 v[52:55], v[228:231], v[88:91], v[52:55]\n"
      "v_fma_f32 v22, v22, s55, v105\n"
      "v_cvt_pk_bf16_f32 v18, v20, v21\n"
      "v_mfma_f32_16x16x32_bf16 v[56:59], v[232:235], v[88:91], v[56:59]\n"
      "s_waitcnt lgkmcnt(3)\n"
      "v_exp_f32 v22, v22\n"
      "v_mfma_f32_16x16x32_bf16 v[56:59], v[248:251], v[92:95], v[56:59]\n"
      "s_cmp_lg_u32 s62, 0\n"
      "v_add_f32 v106, v106, v22\n"
      "v_fma_f32 v23, v23, s55, v105\n"
      "v_mfma_f32_16x16x32_bf16 v[60:63], v[252:255], v[92:95], v[60:63]\n"
      "v_exp_f32 v23, v23\n"
      "v_mfma_f32_16x16x32_bf16 v[60:63], v[236:239], v[88:91], v[60:63]\n"
      "v_add_f32 v106, v106, v23\n"
      "v_cvt_pk_bf16_f32 v19, v22, v23\n"
      "s_cbranch_scc1 1b\n"
      "v_cvt_u32_f32 v110, v106\n"
      "ds_write_b128 v99, v[16:19] offset:5120\n"
      "ds_add_u32 v104, v110 offset:1536\n"
      "s_waitcnt lgkmcnt(2)\n"
      "s_waitcnt lgkmcnt(0)\n"
      "s_barrier\n"
      "ds_read_b128 v[64:67], v98 offset:3072\n"
      "ds_read_b128 v[68:71], v98 offset:4096\n"
      "ds_read_b128 v[72:75], v98 offset:5120\n"
      "ds_read_b128 v[76:79], v98 offset:11520\n"
      "s_waitcnt lgkmcnt(3)\n"
      "v_mfma_f32_16x16x32_bf16 v[0:3], a[192:195], v[64:67], 0\n"
      "ds_read_b32 v109, v104 offset:64\n"
      "s_waitcnt lgkmcnt(3)\n"
      "v_mfma_f32_16x16x32_bf16 v[0:3], a[208:211], v[68:71], v[0:3]\n"
      "ds_read_b128 v[80:83], v98 offset:12544\n"
      "v_mfma_f32_16x16x32_bf16 v[4:7], a[212:215], v[68:71], 0\n"
      "ds_read_b128 v[84:87], v98 offset:13568\n"
      "v_mfma_f32_16x16x32_bf16 v[4:7], a[196:199], v[64:67], v[4:7]\n"
      "ds_read_b32 v108, v104 offset:1536\n"
      "v_mfma_f32_16x16x32_bf16 v[8:11], a[200:203], v[64:67], 0\n"
      "v_mfma_f32_16x16x32_bf16 v[8:11], a[216:219], v[68:71], v[8:11]\n"
      "v_mfma_f32_16x16x32_bf16 v[12:15], a[220:223], v[68:71], 0\n"
      "s_waitcnt lgkmcnt(3)\n"
      "v_fma_f32 v105, v109, s56, v111\n"
      "v_mfma_f32_16x16x32_bf16 v[12:15], a[204:207], v[64:67], v[12:15]\n"
      "v_fma_f32 v24, v24, s55, v105\n"
      "v_fma_f32 v25, v25, s55, v105\n"
      "v_mfma_f32_16x16x32_bf16 v[0:3], a[224:227], v[72:75], v[0:3]\n"
      "ds_read_b128 v[88:91], v98 offset:19968\n"
      "v_exp_f32 v24, v24\n"
      "v_mfma_f32_16x16x32_bf16 v[0:3], a[240:243], v[76:79], v[0:3]\n"
      "v_exp_f32 v25, v25\n"
      "v_mfma_f32_16x16x32_bf16 v[4:7], a[244:247], v[76:79], v[4:7]\n"
      "ds_read_b128 v[92:95], v98 offset:20992\n"
      "v_add_f32 v106, v24, v25\n"
      "v_mfma_f32_16x16x32_bf16 v[4:7], a[228:231], v[72:75], v[4:7]\n"
      "s_waitcnt lgkmcnt(2)\n"
      "v_cvt_pk_bf16_f32 v24, v24, v25\n"
      "v_fma_f32 v26, v26, s55, v105\n"
      "v_mfma_f32_16x16x32_bf16 v[8:11], a[232:235], v[72:75], v[8:11]\n"
      "v_exp_f32 v26, v26\n"
      "v_fma_f32 v27, v27, s55, v105\n"
      "v_mfma_f32_16x16x32_bf16 v[8:11], a[248:251], v[76:79], v[8:11]\n"
      "v_add_f32 v106, v106, v26\n"
      "v_exp_f32 v27, v27\n"
      "v_mfma_f32_16x16x32_bf16 v[12:15], a[252:255], v[76:79], v[12:15]\n"
      "v_add_f32 v106, v106, v27\n"
      "v_cvt_pk_bf16_f32 v25, v26, v27\n"
      "v_fma_f32 v28, v28, s55, v105\n"
      "v_mfma_f32_16x16x32_bf16 v[12:15], a[236:239], v[72:75], v[12:15]\n"
      "v_exp_f32 v28, v28\n"
      "v_fma_f32 v29, v29, s55, v105\n"
      "v_mfma_f32_16x16x32_bf16 v[0:3], v[128:131], v[80:83], v[0:3]\n"
      "ds_read_b128 v[64:67], v98 offset:22016\n"
      "v_add_f32 v106, v106, v28\n"
      "v_exp_f32 v29, v29\n"
      "v_mfma_f32_16x16x32_bf16 v[0:3], v[144:147], v[84:87], v[0:3]\n"
      "v_add_f32 v106, v106, v29\n"
      "v_cvt_pk_bf16_f32 v26, v28, v29\n"
      "v_fma_f32 v30, v30, s55, v105\n"
      "v_mfma_f32_16x16x32_bf16 v[4:7], v[148:151], v[84:87], v[4:7]\n"
      "ds_read_b128 v[68:71], v98 offset:28416\n"
      "v_exp_f32 v30, v30\n"
      "v_fma_f32 v31, v31, s55, v105\n"
      "v_mfma_f32_16x16x32_bf16 v[4:7], v[132:135], v[80:83], v[4:7]\n"
      "v_add_f32 v106, v106, v30\n"
      "v_exp_f32 v31, v31\n"
      "v_mfma_f32_16x16x32_bf16 v[8:11], v[136:139], v[80:83], v[8:11]\n"
      "v_add_f32 v106, v106, v31\n"
      "v_cvt_pk_bf16_f32 v27, v30, v31\n"
      "v_fma_f32 v32, v32, s55, v105\n"
      "v_mfma_f32_16x16x32_bf16 v[8:11], v[152:155], v[84:87], v[8:11]\n"
      "ds_write_b128 v99, v[24:27]\n"
      "v_exp_f32 v32, v32\n"
      "v_fma_f32 v33, v33, s55, v105\n"
      "v_mfma_f32_16x16x32_bf16 v[12:15], v[156:159], v[84:87], v[12:15]\n"
      "v_add_f32 v106, v106, v32\n"
      "v_exp_f32 v33, v33\n"
      "v_mfma_f32_16x16x32_bf16 v[12:15], v[140:143], v[80:83], v[12:15]\n"
      "v_add_f32 v106, v106, v33\n"
      "v_cvt_pk_bf16_f32 v32, v32, v33\n"
      "v_fma_f32 v34, v34, s55, v105\n"
      "s_waitcnt lgkmcnt(4)\n"
      "v_mfma_f32_16x16x32_bf16 v[0:3], v[160:163], v[88:91], v[0:3]\n"
      "ds_read_b128 v[72:75], v98 offset:29440\n"
      "v_exp_f32 v34, v34\n"
      "v_fma_f32 v35, v35, s55, v105\n"
      "s_waitcnt lgkmcnt(4)\n"
      "v_mfma_f32_16x16x32_bf16 v[0:3], v[176:179], v[92:95], v[0:3]\n"
      "v_add_f32 v106, v106, v34\n"
      "v_exp_f32 v35, v35\n"
      "v_mfma_f32_16x16x32_bf16 v[4:7], v[180:183], v[92:95], v[4:7]\n"
      "ds_read_b128 v[76:79], v98 offset:30464\n"
      "v_add_f32 v106, v106, v35\n"
      "v_cvt_pk_bf16_f32 v33, v34, v35\n"
      "v_fma_f32 v36, v36, s55, v105\n"
      "v_mfma_f32_16x16x32_bf16 v[4:7], v[164:167], v[88:91], v[4:7]\n"
      "v_exp_f32 v36, v36\n"
      "v_mfma_f32_16x16x32_bf16 v[8:11], v[168:171], v[88:91], v[8:11]\n"
      "v_add_f32 v106, v106, v36\n"
      "v_fma_f32 v37, v37, s55, v105\n"
      "v_mfma_f32_16x16x32_bf16 v[8:11], v[184:187], v[92:95], v[8:11]\n"
      "v_exp_f32 v37, v37\n"
      "v_fma_f32 v38, v38, s55, v105\n"
      "v_mfma_f32_16x16x32_bf16 v[12:15], v[188:191], v[92:95], v[12:15]\n"
      "v_add_f32 v106, v106, v37\n"
      "v_cvt_pk_bf16_f32 v34, v36, v37\n"
      "v_exp_f32 v38, v38\n"
      "v_mfma_f32_16x16x32_bf16 v[12:15], v[172:175], v[88:91], v[12:15]\n"
      "v_add_f32 v106, v106, v38\n"
      "v_fma_f32 v39, v39, s55, v105\n"
      "s_waitcnt lgkmcnt(4)\n"
      "v_mfma_f32_16x16x32_bf16 v[0:3], v[192:195], v[64:67], v[0:3]\n"
      "v_exp_f32 v39, v39\n"
      "v_fma_f32 v40, v40, s55, v105\n"
      "s_waitcnt lgkmcnt(3)\n"
      "v_mfma_f32_16x16x32_bf16 v[0:3], v[208:211], v[68:71], v[0:3]\n"
      "v_add_f32 v106, v106, v39\n"
      "v_cvt_pk_bf16_f32 v35, v38, v39\n"
      "v_exp_f32 v40, v40\n"
      "v_mfma_f32_16x16x32_bf16 v[4:7], v[212:215], v[68:71], v[4:7]\n"
      "ds_write_b128 v99, v[32:35] offset:1024\n"
      "v_add_f32 v106, v106, v40\n"
      "v_fma_f32 v41, v41, s55, v105\n"
      "v_mfma_f32_16x16x32_bf16 v[4:7], v[196:199], v[64:67], v[4:7]\n"
      "v_exp_f32 v41, v41\n"
      "v_fma_f32 v42, v42, s55, v105\n"
      "v_mfma_f32_16x16x32_bf16 v[8:11], v[200:203], v[64:67], v[8:11]\n"
      "v_add_f32 v106, v106, v41\n"
      "v_cvt_pk_bf16_f32 v40, v40, v41\n"
      "v_exp_f32 v42, v42\n"
      "v_mfma_f32_16x16x32_bf16 v[8:11], v[216:219], v[68:71], v[8:11]\n"
      "v_add_f32 v106, v106, v42\n"
      "v_fma_f32 v43, v43, s55, v105\n"
      "v_mfma_f32_16x16x32_bf16 v[12:15], v[220:223], v[68:71], v[12:15]\n"
      "v_exp_f32 v43, v43\n"
      "v_fma_f32 v44, v44, s55, v105\n"
      "v_mfma_f32_16x16x32_bf16 v[12:15], v[204:207], v[64:67], v[12:15]\n"
      "v_add_f32 v106, v106, v43\n"
      "v_cvt_pk_bf16_f32 v41, v42, v43\n"
      "v_exp_f32 v44, v44\n"
      "s_waitcnt lgkmcnt(2)\n"
      "v_mfma_f32_16x16x32_bf16 v[0:3], v[224:227], v[72:75], v[0:3]\n"
      "v_add_f32 v106, v106, v44\n"
      "v_fma_f32 v45, v45, s55, v105\n"
      "s_waitcnt lgkmcnt(1)\n"
      "v_mfma_f32_16x16x32_bf16 v[0:3], v[240:243], v[76:79], v[0:3]\n"
      "v_exp_f32 v45, v45\n"
      "v_fma_f32 v46, v46, s55, v105\n"
      "v_mfma_f32_16x16x32_bf16 v[4:7], v[244:247], v[76:79], v[4:7]\n"
      "v_add_f32 v106, v106, v45\n"
      "v_cvt_pk_bf16_f32 v42, v44, v45\n"
      "v_exp_f32 v46, v46\n"
      "v_mfma_f32_16x16x32_bf16 v[4:7], v[228:231], v[72:75], v[4:7]\n"
      "v_add_f32 v106, v106, v46\n"
      "v_fma_f32 v47, v47, s55, v105\n"
      "v_mfma_f32_16x16x32_bf16 v[8:11], v[232:235], v[72:75], v[8:11]\n"
      "v_exp_f32 v47, v47\n"
      "v_mfma_f32_16x16x32_bf16 v[8:11], v[248:251], v[76:79], v[8:11]\n"
      "v_add_f32 v106, v106, v47\n"
      "v_cvt_pk_bf16_f32 v43, v46, v47\n"
      "v_cvt_f32_u32 v108, v108\n"
      "v_mfma_f32_16x16x32_bf16 v[12:15], v[252:255], v[76:79], v[12:15]\n"
      "ds_write_b128 v99, v[40:43] offset:2048\n"
      "v_cvt_u32_f32 v110, v106\n"
      "v_rcp_f32 v108, v108\n"
      "v_mfma_f32_16x16x32_bf16 v[12:15], v[236:239], v[72:75], v[12:15]\n"
      "ds_add_u32 v104, v110 offset:1600\n"
      "s_waitcnt lgkmcnt(0)\n"
      "s_barrier\n"
      "ds_read_b128 v[80:83], v98\n"
      "ds_read_b128 v[84:87], v98 offset:1024\n"
      "ds_read_b128 v[88:91], v98 offset:2048\n"
      "ds_read_b128 v[92:95], v98 offset:8448\n"
      "s_waitcnt lgkmcnt(3)\n"
      "v_mfma_f32_16x16x32_bf16 v[24:27], a[192:195], v[80:83], 0\n"
      "ds_read_b128 v[64:67], v98 offset:9472\n"
      "v_mul_f32 v48, v107, v48\n"
      "s_waitcnt lgkmcnt(3)\n"
      "v_mfma_f32_16x16x32_bf16 v[24:27], a[208:211], v[84:87], v[24:27]\n"
      "ds_read_b32 v109, v104 offset:1600\n"
      "v_mul_f32 v49, v107, v49\n"
      "v_mfma_f32_16x16x32_bf16 v[28:31], a[212:215], v[84:87], 0\n"
      "ds_read_b128 v[68:71], v98 offset:10496\n"
      "v_cvt_pk_bf16_f32 v48, v48, v49\n"
      "v_mfma_f32_16x16x32_bf16 v[28:31], a[196:199], v[80:83], v[28:31]\n"
      "v_mul_f32 v50, v107, v50\n"
      "v_mul_f32 v51, v107, v51\n"
      "v_mfma_f32_16x16x32_bf16 v[32:35], a[200:203], v[80:83], 0\n"
      "v_cvt_pk_bf16_f32 v49, v50, v51\n"
      "v_mul_f32 v52, v107, v52\n"
      "v_mfma_f32_16x16x32_bf16 v[32:35], a[216:219], v[84:87], v[32:35]\n"
      "v_mul_f32 v53, v107, v53\n"
      "v_cvt_pk_bf16_f32 v50, v52, v53\n"
      "v_mfma_f32_16x16x32_bf16 v[36:39], a[220:223], v[84:87], 0\n"
      "v_mul_f32 v54, v107, v54\n"
      "v_mul_f32 v55, v107, v55\n"
      "v_mfma_f32_16x16x32_bf16 v[36:39], a[204:207], v[80:83], v[36:39]\n"
      "v_cvt_pk_bf16_f32 v51, v54, v55\n"
      "v_mul_f32 v56, v107, v56\n"
      "s_waitcnt lgkmcnt(4)\n"
      "v_mfma_f32_16x16x32_bf16 v[24:27], a[224:227], v[88:91], v[24:27]\n"
      "ds_read_b128 v[72:75], v98 offset:16896\n"
      "v_mul_f32 v57, v107, v57\n"
      "s_waitcnt lgkmcnt(4)\n"
      "v_mfma_f32_16x16x32_bf16 v[24:27], a[240:243], v[92:95], v[24:27]\n"
      "ds_write_b128 v100, v[48:51]\n"
      "v_mfma_f32_16x16x32_bf16 v[28:31], a[244:247], v[92:95], v[28:31]\n"
      "ds_read_b128 v[76:79], v98 offset:17920\n"
      "v_cvt_pk_bf16_f32 v52, v56, v57\n"
      "v_mfma_f32_16x16x32_bf16 v[28:31], a[228:231], v[88:91], v[28:31]\n"
      "s_waitcnt lgkmcnt(4)\n"
      "v_mul_f32 v58, v107, v58\n"
      "v_mul_f32 v59, v107, v59\n"
      "v_mfma_f32_16x16x32_bf16 v[32:35], a[232:235], v[88:91], v[32:35]\n"
      "v_cvt_pk_bf16_f32 v53, v58, v59\n"
      "v_mul_f32 v60, v107, v60\n"
      "v_mfma_f32_16x16x32_bf16 v[32:35], a[248:251], v[92:95], v[32:35]\n"
      "v_mul_f32 v61, v107, v61\n"
      "v_cvt_pk_bf16_f32 v54, v60, v61\n"
      "v_mfma_f32_16x16x32_bf16 v[36:39], a[252:255], v[92:95], v[36:39]\n"
      "v_mul_f32 v62, v107, v62\n"
      "v_mul_f32 v63, v107, v63\n"
      "v_mfma_f32_16x16x32_bf16 v[36:39], a[236:239], v[88:91], v[36:39]\n"
      "v_cvt_pk_bf16_f32 v55, v62, v63\n"
      "v_mul_f32 v0, v108, v0\n"
      "v_mfma_f32_16x16x32_bf16 v[24:27], v[128:131], v[64:67], v[24:27]\n"
      "ds_read_b128 v[80:83], v98 offset:18944\n"
      "v_mul_f32 v1, v108, v1\n"
      "s_waitcnt lgkmcnt(4)\n"
      "v_mfma_f32_16x16x32_bf16 v[24:27], v[144:147], v[68:71], v[24:27]\n"
      "ds_write_b128 v100, v[52:55] offset:16\n"
      "v_mfma_f32_16x16x32_bf16 v[28:31], v[148:151], v[68:71], v[28:31]\n"
      "ds_read_b128 v[84:87], v98 offset:25344\n"
      "v_cvt_pk_bf16_f32 v0, v0, v1\n"
      "v_mfma_f32_16x16x32_bf16 v[28:31], v[132:135], v[64:67], v[28:31]\n"
      "ds_read_b128 v[56:59], v101\n"
      "v_mul_f32 v2, v108, v2\n"
      "v_mfma_f32_16x16x32_bf16 v[32:35], v[136:139], v[64:67], v[32:35]\n"
      "ds_read_b128 v[60:63], v101 offset:1152\n"
      "v_mul_f32 v3, v108, v3\n"
      "v_mfma_f32_16x16x32_bf16 v[32:35], v[152:155], v[68:71], v[32:35]\n"
      "v_cvt_pk_bf16_f32 v1, v2, v3\n"
      "v_mul_f32 v4, v108, v4\n"
      "v_mfma_f32_16x16x32_bf16 v[36:39], v[156:159], v[68:71], v[36:39]\n"
      "v_mul_f32 v5, v108, v5\n"
      "v_cvt_pk_bf16_f32 v2, v4, v5\n"
      "v_mfma_f32_16x16x32_bf16 v[36:39], v[140:143], v[64:67], v[36:39]\n"
      "v_mul_f32 v6, v108, v6\n"
      "v_mul_f32 v7, v108, v7\n"
      "s_waitcnt lgkmcnt(7)\n"
      "v_mfma_f32_16x16x32_bf16 v[24:27], v[160:163], v[72:75], v[24:27]\n"
      "ds_read_b128 v[88:91], v98 offset:26368\n"
      "v_cvt_pk_bf16_f32 v3, v6, v7\n"
      "s_waitcnt lgkmcnt(6)\n"
      "v_mfma_f32_16x16x32_bf16 v[24:27], v[176:179], v[76:79], v[24:27]\n"
      "ds_write_b128 v100, v[0:3]\n"
      "v_mfma_f32_16x16x32_bf16 v[28:31], v[180:183], v[76:79], v[28:31]\n"
      "ds_read_b128 v[92:95], v98 offset:27392\n"
      "v_mul_f32 v8, v108, v8\n"
      "v_mfma_f32_16x16x32_bf16 v[28:31], v[164:167], v[72:75], v[28:31]\n"
      "s_waitcnt lgkmcnt(3)\n"
      "v_mul_f32 v9, v108, v9\n"
      "v_cvt_pk_bf16_f32 v4, v8, v9\n"
      "v_mfma_f32_16x16x32_bf16 v[32:35], v[168:171], v[72:75], v[32:35]\n"
      "buffer_store_dwordx4 v[56:59], v103, s[48:51], s60 offen sc1\n"
      "v_mul_f32 v10, v108, v10\n"
      "v_mfma_f32_16x16x32_bf16 v[32:35], v[184:187], v[76:79], v[32:35]\n"
      "buffer_store_dwordx4 v[60:63], v103, s[48:51], s61 offen sc1\n"
      "v_mul_f32 v11, v108, v11\n"
      "v_mfma_f32_16x16x32_bf16 v[36:39], v[188:191], v[76:79], v[36:39]\n"
      "s_add_u32 s60, s60, 8192\n"
      "v_cvt_pk_bf16_f32 v5, v10, v11\n"
      "v_mul_f32 v12, v108, v12\n"
      "v_mfma_f32_16x16x32_bf16 v[36:39], v[172:175], v[72:75], v[36:39]\n"
      "s_add_u32 s61, s61, 8192\n"
      "v_mul_f32 v13, v108, v13\n"
      "v_cvt_pk_bf16_f32 v6, v12, v13\n"
      "v_mfma_f32_16x16x32_bf16 v[24:27], v[192:195], v[80:83], v[24:27]\n"
      "v_mul_f32 v14, v108, v14\n"
      "v_mul_f32 v15, v108, v15\n"
      "v_mfma_f32_16x16x32_bf16 v[24:27], v[208:211], v[84:87], v[24:27]\n"
      "v_cvt_pk_bf16_f32 v7, v14, v15\n"
      "v_cvt_f32_u32 v109, v109\n"
      "v_mfma_f32_16x16x32_bf16 v[28:31], v[212:215], v[84:87], v[28:31]\n"
      "ds_write_b128 v100, v[4:7] offset:16\n"
      "v_mfma_f32_16x16x32_bf16 v[28:31], v[196:199], v[80:83], v[28:31]\n"
      "ds_read_b128 v[8:11], v101\n"
      "v_mfma_f32_16x16x32_bf16 v[32:35], v[200:203], v[80:83], v[32:35]\n"
      "ds_read_b128 v[12:15], v101 offset:1152\n"
      "v_mfma_f32_16x16x32_bf16 v[32:35], v[216:219], v[84:87], v[32:35]\n"
      "v_rcp_f32 v109, v109\n"
      "v_mfma_f32_16x16x32_bf16 v[36:39], v[220:223], v[84:87], v[36:39]\n"
      "v_mfma_f32_16x16x32_bf16 v[36:39], v[204:207], v[80:83], v[36:39]\n"
      "s_waitcnt lgkmcnt(5)\n"
      "v_mfma_f32_16x16x32_bf16 v[24:27], v[224:227], v[88:91], v[24:27]\n"
      "s_waitcnt lgkmcnt(3)\n"
      "v_mfma_f32_16x16x32_bf16 v[24:27], v[240:243], v[92:95], v[24:27]\n"
      "v_mfma_f32_16x16x32_bf16 v[28:31], v[244:247], v[92:95], v[28:31]\n"
      "s_waitcnt lgkmcnt(0)\n"
      "v_mfma_f32_16x16x32_bf16 v[28:31], v[228:231], v[88:91], v[28:31]\n"
      "buffer_store_dwordx4 v[8:11], v103, s[48:51], s60 offen sc1\n"
      "v_mfma_f32_16x16x32_bf16 v[32:35], v[232:235], v[88:91], v[32:35]\n"
      "buffer_store_dwordx4 v[12:15], v103, s[48:51], s61 offen sc1\n"
      "v_mfma_f32_16x16x32_bf16 v[32:35], v[248:251], v[92:95], v[32:35]\n"
      "s_add_u32 s60, s60, 8192\n"
      "v_mfma_f32_16x16x32_bf16 v[36:39], v[252:255], v[92:95], v[36:39]\n"
      "s_add_u32 s61, s61, 8192\n"
      "v_mfma_f32_16x16x32_bf16 v[36:39], v[236:239], v[88:91], v[36:39]\n"
      "v_mul_f32 v24, v109, v24\n"
      "v_mul_f32 v25, v109, v25\n"
      "v_cvt_pk_bf16_f32 v24, v24, v25\n"
      "v_mul_f32 v26, v109, v26\n"
      "v_mul_f32 v27, v109, v27\n"
      "v_cvt_pk_bf16_f32 v25, v26, v27\n"
      "v_mul_f32 v28, v109, v28\n"
      "v_mul_f32 v29, v109, v29\n"
      "v_cvt_pk_bf16_f32 v26, v28, v29\n"
      "v_mul_f32 v30, v109, v30\n"
      "v_mul_f32 v31, v109, v31\n"
      "v_cvt_pk_bf16_f32 v27, v30, v31\n"
      "v_mul_f32 v32, v109, v32\n"
      "v_mul_f32 v33, v109, v33\n"
      "v_cvt_pk_bf16_f32 v28, v32, v33\n"
      "v_mul_f32 v34, v109, v34\n"
      "v_mul_f32 v35, v109, v35\n"
      "v_cvt_pk_bf16_f32 v29, v34, v35\n"
      "v_mul_f32 v36, v109, v36\n"
      "v_mul_f32 v37, v109, v37\n"
      "v_cvt_pk_bf16_f32 v30, v36, v37\n"
      "v_mul_f32 v38, v109, v38\n"
      "v_mul_f32 v39, v109, v39\n"
      "v_cvt_pk_bf16_f32 v31, v38, v39\n"
      "ds_write_b128 v100, v[24:27]\n"
      "ds_write_b128 v100, v[28:31] offset:16\n"
      "ds_read_b128 v[32:35], v101\n"
      "ds_read_b128 v[36:39], v101 offset:1152\n"
      "s_waitcnt lgkmcnt(0)\n"
      "buffer_store_dwordx4 v[32:35], v103, s[48:51], s60 offen sc1\n"
      "buffer_store_dwordx4 v[36:39], v103, s[48:51], s61 offen sc1\n"
      "s_add_u32 s60, s60, 8192\n"
      "s_add_u32 s61, s61, 8192\n"
      "s_branch 9f\n"
      ".p2align 6\n"
      "7:\n"
      "v_mfma_f32_16x16x32_bf16 v[24:27], a[0:3], v[80:83], 0\n"
      "ds_read_b128 v[64:67], v96 offset:256\n"
      "v_cvt_u32_f32 v110, v106\n"
      "v_mfma_f32_16x16x32_bf16 v[24:27], a[4:7], v[84:87], v[24:27]\n"
      "ds_write_b128 v99, v[40:43] offset:2048\n"
      "ds_add_u32 v104, v110 offset:1600\n"
      "v_mfma_f32_16x16x32_bf16 v[28:31], a[36:39], v[84:87], 0\n"
      "ds_read_b128 v[68:71], v96 offset:320\n"
      "v_mul_f32 v48, v107, v48\n"
      "v_mfma_f32_16x16x32_bf16 v[28:31], a[32:35], v[80:83], v[28:31]\n"
      "v_mul_f32 v49, v107, v49\n"
      "v_cvt_pk_bf16_f32 v48, v48, v49\n"
      "v_mfma_f32_16x16x32_bf16 v[32:35], a[64:67], v[80:83], 0\n"
      "v_mul_f32 v50, v107, v50\n"
      "v_mul_f32 v51, v107, v51\n"
      "v_mfma_f32_16x16x32_bf16 v[32:35], a[68:71], v[84:87], v[32:35]\n"
      "v_cvt_pk_bf16_f32 v49, v50, v51\n"
      "v_mul_f32 v52, v107, v52\n"
      "v_mfma_f32_16x16x32_bf16 v[36:39], a[100:103], v[84:87], 0\n"
      "v_mul_f32 v53, v107, v53\n"
      "v_cvt_pk_bf16_f32 v50, v52, v53\n"
      "v_mfma_f32_16x16x32_bf16 v[36:39], a[96:99], v[80:83], v[36:39]\n"
      "v_mul_f32 v54, v107, v54\n"
      "v_mul_f32 v55, v107, v55\n"
      "v_mfma_f32_16x16x32_bf16 v[40:43], a[128:131], v[80:83], 0\n"
      "v_cvt_pk_bf16_f32 v51, v54, v55\n"
      "v_mul_f32 v56, v107, v56\n"
      "v_mfma_f32_16x16x32_bf16 v[40:43], a[132:135], v[84:87], v[40:43]\n"
      "ds_write_b128 v100, v[48:51]\n"
      "v_mfma_f32_16x16x32_bf16 v[44:47], a[164:167], v[84:87], 0\n"
      "s_waitcnt vmcnt(12) lgkmcnt(2)\n"
      "s_barrier\n"
      "v_mul_f32 v57, v107, v57\n"
      "v_cvt_pk_bf16_f32 v52, v56, v57\n"
      "v_mfma_f32_16x16x32_bf16 v[44:47], a[160:163], v[80:83], v[44:47]\n"
      "ds_read_b32 v109, v104 offset:128\n"
      "v_mul_f32 v58, v107, v58\n"
      "v_mfma_f32_16x16x32_bf16 v[24:27], a[8:11], v[88:91], v[24:27]\n"
      "ds_read_b128 v[72:75], v96 offset:384\n"
      "v_mul_f32 v59, v107, v59\n"
      "v_mfma_f32_16x16x32_bf16 v[24:27], a[12:15], v[92:95], v[24:27]\n"
      "s_add_u32 m0, s53, 6336\n"
      "v_cvt_pk_bf16_f32 v53, v58, v59\n"
      "v_mul_f32 v60, v107, v60\n"
      "v_mfma_f32_16x16x32_bf16 v[28:31], a[44:47], v[92:95], v[28:31]\n"
      "ds_read_b128 v[76:79], v96 offset:448\n"
      "v_mul_f32 v61, v107, v61\n"
      "v_mfma_f32_16x16x32_bf16 v[28:31], a[40:43], v[88:91], v[28:31]\n"
      "ds_read_b32 v112, v104 offset:1600\n"
      "v_cvt_pk_bf16_f32 v54, v60, v61\n"
      "v_mfma_f32_16x16x32_bf16 v[32:35], a[72:75], v[88:91], v[32:35]\n"
      "v_mul_f32 v62, v107, v62\n"
      "v_mul_f32 v63, v107, v63\n"
      "v_mfma_f32_16x16x32_bf16 v[32:35], a[76:79], v[92:95], v[32:35]\n"
      "s_waitcnt lgkmcnt(3)\n"
      "v_cvt_pk_bf16_f32 v55, v62, v63\n"
      "v_fma_f32 v105, v109, s56, v111\n"
      "v_mfma_f32_16x16x32_bf16 v[36:39], a[108:111], v[92:95], v[36:39]\n"
      "ds_write_b128 v100, v[52:55] offset:16\n"
      "v_mfma_f32_16x16x32_bf16 v[36:39], a[104:107], v[88:91], v[36:39]\n"
      "ds_read_b128 v[56:59], v101\n"
      "v_fma_f32 v0, v0, s55, v105\n"
      "v_mfma_f32_16x16x32_bf16 v[40:43], a[136:139], v[88:91], v[40:43]\n"
      "v_exp_f32 v0, v0\n"
      "v_mfma_f32_16x16x32_bf16 v[40:43], a[140:143], v[92:95], v[40:43]\n"
      "ds_read_b128 v[60:63], v101 offset:1152\n"
      "v_fma_f32 v1, v1, s55, v105\n"
      "v_mfma_f32_16x16x32_bf16 v[44:47], a[172:175], v[92:95], v[44:47]\n"
      "s_mov_b32 s59, 0\n"
      "v_exp_f32 v1, v1\n"
      "v_mfma_f32_16x16x32_bf16 v[44:47], a[168:171], v[88:91], v[44:47]\n"
      "s_waitcnt lgkmcnt(3)\n"
      "v_add_f32 v106, v0, v1\n"
      "v_cvt_pk_bf16_f32 v0, v0, v1\n"
      "v_mfma_f32_16x16x32_bf16 v[24:27], a[16:19], v[64:67], v[24:27]\n"
      "ds_read_b128 v[80:83], v98\n"
      "v_fma_f32 v2, v2, s55, v105\n"
      "v_mfma_f32_16x16x32_bf16 v[24:27], a[20:23], v[68:71], v[24:27]\n"
      "v_exp_f32 v2, v2\n"
      "v_mfma_f32_16x16x32_bf16 v[28:31], a[52:55], v[68:71], v[28:31]\n"
      "ds_read_b128 v[84:87], v98 offset:1024\n"
      "v_add_f32 v106, v106, v2\n"
      "v_mfma_f32_16x16x32_bf16 v[28:31], a[48:51], v[64:67], v[28:31]\n"
      "s_waitcnt lgkmcnt(2)\n"
      "v_fma_f32 v3, v3, s55, v105\n"
      "v_fma_f32 v4, v4, s55, v105\n"
      "v_mfma_f32_16x16x32_bf16 v[32:35], a[80:83], v[64:67], v[32:35]\n"
      "buffer_store_dwordx4 v[56:59], v103, s[48:51], s60 offen sc1\n"
      "v_cvt_f32_u32 v112, v112\n"
      "v_mfma_f32_16x16x32_bf16 v[32:35], a[84:87], v[68:71], v[32:35]\n"
      "s_add_u32 s60, s60, 8192\n"
      "v_exp_f32 v3, v3\n"
      "v_mfma_f32_16x16x32_bf16 v[36:39], a[116:119], v[68:71], v[36:39]\n"
      "v_add_f32 v106, v106, v3\n"
      "v_cvt_pk_bf16_f32 v1, v2, v3\n"
      "v_mfma_f32_16x16x32_bf16 v[36:39], a[112:115], v[64:67], v[36:39]\n"
      "v_exp_f32 v4, v4\n"
      "v_mfma_f32_16x16x32_bf16 v[40:43], a[144:147], v[64:67], v[40:43]\n"
      "v_add_f32 v106, v106, v4\n"
      "v_fma_f32 v5, v5, s55, v105\n"
      "v_mfma_f32_16x16x32_bf16 v[40:43], a[148:151], v[68:71], v[40:43]\n"
      "buffer_store_dwordx4 v[60:63], v103, s[48:51], s61 offen sc1\n"
      "v_fma_f32 v6, v6, s55, v105\n"
      "v_mfma_f32_16x16x32_bf16 v[44:47], a[180:183], v[68:71], v[44:47]\n"
      "s_add_u32 s61, s61, 8192\n"
      "v_rcp_f32 v107, v112\n"
      "v_mfma_f32_16x16x32_bf16 v[44:47], a[176:179], v[64:67], v[44:47]\n"
      "v_exp_f32 v5, v5\n"
      "v_mfma_f32_16x16x32_bf16 v[24:27], a[24:27], v[72:75], v[24:27]\n"
      "ds_read_b128 v[88:91], v98 offset:2048\n"
      "v_add_f32 v106, v106, v5\n"
      "v_mfma_f32_16x16x32_bf16 v[24:27], a[28:31], v[76:79], v[24:27]\n"
      "v_fma_f32 v7, v7, s55, v105\n"
      "v_cvt_pk_bf16_f32 v2, v4, v5\n"
      "v_mfma_f32_16x16x32_bf16 v[28:31], a[60:63], v[76:79], v[28:31]\n"
      "ds_read_b128 v[92:95], v98 offset:8448\n"
      "v_fma_f32 v8, v8, s55, v105\n"
      "v_mfma_f32_16x16x32_bf16 v[28:31], a[56:59], v[72:75], v[28:31]\n"
      "v_exp_f32 v6, v6\n"
      "v_mfma_f32_16x16x32_bf16 v[32:35], a[88:91], v[72:75], v[32:35]\n"
      "v_add_f32 v106, v106, v6\n"
      "v_fma_f32 v9, v9, s55, v105\n"
      "v_mfma_f32_16x16x32_bf16 v[32:35], a[92:95], v[76:79], v[32:35]\n"
      "v_exp_f32 v7, v7\n"
      "v_mfma_f32_16x16x32_bf16 v[36:39], a[124:127], v[76:79], v[36:39]\n"
      "v_add_f32 v106, v106, v7\n"
      "v_cvt_pk_bf16_f32 v3, v6, v7\n"
      "v_mfma_f32_16x16x32_bf16 v[36:39], a[120:123], v[72:75], v[36:39]\n"
      "ds_write_b128 v99, v[0:3] offset:3072\n"
      "v_mfma_f32_16x16x32_bf16 v[40:43], a[152:155], v[72:75], v[40:43]\n"
      "s_waitcnt lgkmcnt(3)\n"
      "v_exp_f32 v8, v8\n"
      "v_mfma_f32_16x16x32_bf16 v[40:43], a[156:159], v[76:79], v[40:43]\n"
      "v_add_f32 v106, v106, v8\n"
      "v_fma_f32 v10, v10, s55, v105\n"
      "v_mfma_f32_16x16x32_bf16 v[44:47], a[188:191], v[76:79], v[44:47]\n"
      "v_exp_f32 v9, v9\n"
      "v_mfma_f32_16x16x32_bf16 v[44:47], a[184:187], v[72:75], v[44:47]\n"
      "v_add_f32 v106, v106, v9\n"
      "v_cvt_pk_bf16_f32 v8, v8, v9\n"
      "v_mfma_f32_16x16x32_bf16 v[48:51], a[192:195], v[80:83], 0\n"
      "ds_read_b128 v[64:67], v98 offset:9472\n"
      "v_max3_f32 v108, v24, v25, v26\n"
      "v_mfma_f32_16x16x32_bf16 v[48:51], a[208:211], v[84:87], v[48:51]\n"
      "buffer_load_dwordx4 v102, s[36:39], s57 offen nt lds\n"
      "v_mfma_f32_16x16x32_bf16 v[52:55], a[212:215], v[84:87], 0\n"
      "ds_read_b128 v[68:71], v98 offset:10496\n"
      "v_fma_f32 v11, v11, s55, v105\n"
      "v_mfma_f32_16x16x32_bf16 v[52:55], a[196:199], v[80:83], v[52:55]\n"
      "s_add_u32 m0, m0, 544\n"
      "v_exp_f32 v10, v10\n"
      "v_mfma_f32_16x16x32_bf16 v[56:59], a[200:203], v[80:83], 0\n"
      "s_waitcnt lgkmcnt(3)\n"
      "v_add_f32 v106, v106, v10\n"
      "v_max3_f32 v108, v108, v27, v28\n"
      "v_mfma_f32_16x16x32_bf16 v[56:59], a[216:219], v[84:87], v[56:59]\n"
      "buffer_load_dwordx4 v102, s[36:39], s57 offen offset:512 nt lds\n"
      "v_mfma_f32_16x16x32_bf16 v[60:63], a[220:223], v[84:87], 0\n"
      "s_add_u32 s57, s57, 8192\n"
      "v_exp_f32 v11, v11\n"
      "v_mfma_f32_16x16x32_bf16 v[60:63], a[204:207], v[80:83], v[60:63]\n"
      "v_add_f32 v106, v106, v11\n"
      "v_cvt_pk_bf16_f32 v9, v10, v11\n"
      "v_mfma_f32_16x16x32_bf16 v[48:51], a[224:227], v[88:91], v[48:51]\n"
      "ds_read_b128 v[72:75], v98 offset:16896\n"
      "v_fma_f32 v12, v12, s55, v105\n"
      "v_mfma_f32_16x16x32_bf16 v[48:51], a[240:243], v[92:95], v[48:51]\n"
      "v_max3_f32 v108, v108, v29, v30\n"
      "v_max3_f32 v108, v108, v31, v32\n"
      "v_mfma_f32_16x16x32_bf16 v[52:55], a[244:247], v[92:95], v[52:55]\n"
      "ds_read_b128 v[76:79], v98 offset:17920\n"
      "v_fma_f32 v13, v13, s55, v105\n"
      "v_mfma_f32_16x16x32_bf16 v[52:55], a[228:231], v[88:91], v[52:55]\n"
      "v_exp_f32 v12, v12\n"
      "v_mfma_f32_16x16x32_bf16 v[56:59], a[232:235], v[88:91], v[56:59]\n"
      "s_waitcnt lgkmcnt(2)\n"
      "v_add_f32 v106, v106, v12\n"
      "v_add_u32 v104, 128, v104\n"
      "v_mfma_f32_16x16x32_bf16 v[56:59], a[248:251], v[92:95], v[56:59]\n"
      "v_exp_f32 v13, v13\n"
      "v_mfma_f32_16x16x32_bf16 v[60:63], a[252:255], v[92:95], v[60:63]\n"
      "v_add_f32 v106, v106, v13\n"
      "v_cvt_pk_bf16_f32 v10, v12, v13\n"
      "v_mfma_f32_16x16x32_bf16 v[60:63], a[236:239], v[88:91], v[60:63]\n"
      "v_fma_f32 v14, v14, s55, v105\n"
      "v_max3_f32 v108, v108, v33, v34\n"
      "v_mfma_f32_16x16x32_bf16 v[48:51], v[128:131], v[64:67], v[48:51]\n"
      "ds_read_b128 v[80:83], v98 offset:18944\n"
      "v_max3_f32 v108, v108, v35, v36\n"
      "v_mfma_f32_16x16x32_bf16 v[48:51], v[144:147], v[68:71], v[48:51]\n"
      "v_exp_f32 v14, v14\n"
      "v_mfma_f32_16x16x32_bf16 v[52:55], v[148:151], v[68:71], v[52:55]\n"
      "ds_read_b128 v[84:87], v98 offset:25344\n"
      "v_add_f32 v106, v106, v14\n"
      "v_mfma_f32_16x16x32_bf16 v[52:55], v[132:135], v[64:67], v[52:55]\n"
      "v_fma_f32 v15, v15, s55, v105\n"
      "v_max3_f32 v108, v108, v37, v38\n"
      "v_mfma_f32_16x16x32_bf16 v[56:59], v[136:139], v[64:67], v[56:59]\n"
      "s_waitcnt lgkmcnt(2)\n"
      "v_exp_f32 v15, v15\n"
      "v_mfma_f32_16x16x32_bf16 v[56:59], v[152:155], v[68:71], v[56:59]\n"
      "v_add_f32 v106, v106, v15\n"
      "v_cvt_pk_bf16_f32 v11, v14, v15\n"
      "v_mfma_f32_16x16x32_bf16 v[60:63], v[156:159], v[68:71], v[60:63]\n"
      "ds_write_b128 v99, v[8:11] offset:4096\n"
      "v_mfma_f32_16x16x32_bf16 v[60:63], v[140:143], v[64:67], v[60:63]\n"
      "v_add_u32 v96, s59, v97\n"
      "v_fma_f32 v16, v16, s55, v105\n"
      "v_mfma_f32_16x16x32_bf16 v[48:51], v[160:163], v[72:75], v[48:51]\n"
      "ds_read_b128 v[88:91], v98 offset:26368\n"
      "v_max3_f32 v108, v108, v39, v40\n"
      "v_mfma_f32_16x16x32_bf16 v[48:51], v[176:179], v[76:79], v[48:51]\n"
      "v_exp_f32 v16, v16\n"
      "v_mfma_f32_16x16x32_bf16 v[52:55], v[180:183], v[76:79], v[52:55]\n"
      "ds_read_b128 v[92:95], v98 offset:27392\n"
      "v_add_f32 v106, v106, v16\n"
      "v_mfma_f32_16x16x32_bf16 v[52:55], v[164:167], v[72:75], v[52:55]\n"
      "v_fma_f32 v17, v17, s55, v105\n"
      "v_max3_f32 v108, v108, v41, v42\n"
      "v_mfma_f32_16x16x32_bf16 v[56:59], v[168:171], v[72:75], v[56:59]\n"
      "s_waitcnt lgkmcnt(3)\n"
      "v_exp_f32 v17, v17\n"
      "v_mfma_f32_16x16x32_bf16 v[56:59], v[184:187], v[76:79], v[56:59]\n"
      "v_add_f32 v106, v106, v17\n"
      "v_cvt_pk_bf16_f32 v16, v16, v17\n"
      "v_mfma_f32_16x16x32_bf16 v[60:63], v[188:191], v[76:79], v[60:63]\n"
      "v_fma_f32 v18, v18, s55, v105\n"
      "v_max3_f32 v108, v108, v43, v44\n"
      "v_mfma_f32_16x16x32_bf16 v[60:63], v[172:175], v[72:75], v[60:63]\n"
      "v_exp_f32 v18, v18\n"
      "v_mfma_f32_16x16x32_bf16 v[48:51], v[192:195], v[80:83], v[48:51]\n"
      "ds_read_b128 v[64:67], v96\n"
      "v_add_f32 v106, v106, v18\n"
      "v_mfma_f32_16x16x32_bf16 v[48:51], v[208:211], v[84:87], v[48:51]\n"
      "v_fma_f32 v19, v19, s55, v105\n"
      "v_max3_f32 v108, v108, v45, v46\n"
      "v_mfma_f32_16x16x32_bf16 v[52:55], v[212:215], v[84:87], v[52:55]\n"
      "ds_read_b128 v[68:71], v96 offset:64\n"
      "v_fma_f32 v20, v20, s55, v105\n"
      "v_mfma_f32_16x16x32_bf16 v[52:55], v[196:199], v[80:83], v[52:55]\n"
      "v_exp_f32 v19, v19\n"
      "v_mfma_f32_16x16x32_bf16 v[56:59], v[200:203], v[80:83], v[56:59]\n"
      "s_waitcnt lgkmcnt(2)\n"
      "v_add_f32 v106, v106, v19\n"
      "v_cvt_pk_bf16_f32 v17, v18, v19\n"
      "v_mfma_f32_16x16x32_bf16 v[56:59], v[216:219], v[84:87], v[56:59]\n"
      "v_exp_f32 v20, v20\n"
      "v_mfma_f32_16x16x32_bf16 v[60:63], v[220:223], v[84:87], v[60:63]\n"
      "v_add_f32 v106, v106, v20\n"
      "v_max_f32 v108, v108, v47\n"
      "v_mfma_f32_16x16x32_bf16 v[60:63], v[204:207], v[80:83], v[60:63]\n"
      "ds_max_f32 v104, v108 offset:64\n"
      "v_mfma_f32_16x16x32_bf16 v[48:51], v[224:227], v[88:91], v[48:51]\n"
      "ds_read_b128 v[72:75], v96 offset:128\n"
      "v_fma_f32 v21, v21, s55, v105\n"
      "v_mfma_f32_16x16x32_bf16 v[48:51], v[240:243], v[92:95], v[48:51]\n"
      "v_exp_f32 v21, v21\n"
      "v_mfma_f32_16x16x32_bf16 v[52:55], v[244:247], v[92:95], v[52:55]\n"
      "ds_read_b128 v[76:79], v96 offset:192\n"
      "v_add_f32 v106, v106, v21\n"
      "v_mfma_f32_16x16x32_bf16 v[52:55], v[228:231], v[88:91], v[52:55]\n"
      "v_fma_f32 v22, v22, s55, v105\n"
      "v_cvt_pk_bf16_f32 v18, v20, v21\n"
      "v_mfma_f32_16x16x32_bf16 v[56:59], v[232:235], v[88:91], v[56:59]\n"
      "s_waitcnt lgkmcnt(3)\n"
      "v_exp_f32 v22, v22\n"
      "v_mfma_f32_16x16x32_bf16 v[56:59], v[248:251], v[92:95], v[56:59]\n"
      "v_add_f32 v106, v106, v22\n"
      "v_fma_f32 v23, v23, s55, v105\n"
      "v_mfma_f32_16x16x32_bf16 v[60:63], v[252:255], v[92:95], v[60:63]\n"
      "v_exp_f32 v23, v23\n"
      "v_mfma_f32_16x16x32_bf16 v[60:63], v[236:239], v[88:91], v[60:63]\n"
      "v_add_f32 v106, v106, v23\n"
      "v_cvt_pk_bf16_f32 v19, v22, v23\n"
      ".p2align 6\n"
      "8:\n"
      "v_mfma_f32_16x16x32_bf16 v[0:3], a[0:3], v[64:67], 0\n"
      "ds_read_b128 v[80:83], v96 offset:256\n"
      "v_cvt_u32_f32 v110, v106\n"
      "v_mfma_f32_16x16x32_bf16 v[0:3], a[4:7], v[68:71], v[0:3]\n"
      "ds_write_b128 v99, v[16:19] offset:5120\n"
      "ds_add_u32 v104, v110 offset:1536\n"
      "s_add_u32 s68, s68, 1\n"
      "v_mfma_f32_16x16x32_bf16 v[4:7], a[36:39], v[68:71], 0\n"
      "ds_read_b128 v[84:87], v96 offset:320\n"
      "v_mul_f32 v48, v107, v48\n"
      "v_mfma_f32_16x16x32_bf16 v[4:7], a[32:35], v[64:67], v[4:7]\n"
      "s_and_b32 s68, s68, 7\n"
      "v_mul_f32 v49, v107, v49\n"
      "v_cvt_pk_bf16_f32 v48, v48, v49\n"
      "v_mfma_f32_16x16x32_bf16 v[8:11], a[64:67], v[64:67], 0\n"
      "s_add_u32 s58, s68, 3\n"
      "v_mul_f32 v50, v107, v50\n"
      "v_mul_f32 v51, v107, v51\n"
      "v_mfma_f32_16x16x32_bf16 v[8:11], a[68:71], v[68:71], v[8:11]\n"
      "s_and_b32 s58, s58, 7\n"
      "v_cvt_pk_bf16_f32 v49, v50, v51\n"
      "v_mul_f32 v52, v107, v52\n"
      "v_mfma_f32_16x16x32_bf16 v[12:15], a[100:103], v[68:71], 0\n"
      "v_mul_f32 v53, v107, v53\n"
      "v_cvt_pk_bf16_f32 v50, v52, v53\n"
      "v_mfma_f32_16x16x32_bf16 v[12:15], a[96:99], v[64:67], v[12:15]\n"
      "s_mul_i32 s58, s58, 2112\n"
      "v_mul_f32 v54, v107, v54\n"
      "v_mul_f32 v55, v107, v55\n"
      "v_mfma_f32_16x16x32_bf16 v[16:19], a[128:131], v[64:67], 0\n"
      "v_cvt_pk_bf16_f32 v51, v54, v55\n"
      "v_mul_f32 v56, v107, v56\n"
      "v_mfma_f32_16x16x32_bf16 v[16:19], a[132:135], v[68:71], v[16:19]\n"
      "ds_write_b128 v100, v[48:51]\n"
      "v_mfma_f32_16x16x32_bf16 v[20:23], a[164:167], v[68:71], 0\n"
      "s_waitcnt vmcnt(8) lgkmcnt(2)\n"
      "s_barrier\n"
      "v_mul_f32 v57, v107, v57\n"
      "v_cvt_pk_bf16_f32 v52, v56, v57\n"
      "v_mfma_f32_16x16x32_bf16 v[20:23], a[160:163], v[64:67], v[20:23]\n"
      "ds_read_b32 v109, v104 offset:64\n"
      "v_mul_f32 v58, v107, v58\n"
      "v_mfma_f32_16x16x32_bf16 v[0:3], a[8:11], v[72:75], v[0:3]\n"
      "ds_read_b128 v[88:91], v96 offset:384\n"
      "v_mul_f32 v59, v107, v59\n"
      "v_mfma_f32_16x16x32_bf16 v[0:3], a[12:15], v[76:79], v[0:3]\n"
      "s_add_u32 m0, s53, s58\n"
      "v_cvt_pk_bf16_f32 v53, v58, v59\n"
      "v_mul_f32 v60, v107, v60\n"
      "v_mfma_f32_16x16x32_bf16 v[4:7], a[44:47], v[76:79], v[4:7]\n"
      "ds_read_b128 v[92:95], v96 offset:448\n"
      "v_mul_f32 v61, v107, v61\n"
      "v_mfma_f32_16x16x32_bf16 v[4:7], a[40:43], v[72:75], v[4:7]\n"
      "ds_read_b32 v112, v104 offset:1536\n"
      "v_cvt_pk_bf16_f32 v54, v60, v61\n"
      "v_mfma_f32_16x16x32_bf16 v[8:11], a[72:75], v[72:75], v[8:11]\n"
      "v_mul_f32 v62, v107, v62\n"
      "v_mul_f32 v63, v107, v63\n"
      "v_mfma_f32_16x16x32_bf16 v[8:11], a[76:79], v[76:79], v[8:11]\n"
      "s_waitcnt lgkmcnt(3)\n"
      "v_cvt_pk_bf16_f32 v55, v62, v63\n"
      "v_fma_f32 v105, v109, s56, v111\n"
      "v_mfma_f32_16x16x32_bf16 v[12:15], a[108:111], v[76:79], v[12:15]\n"
      "ds_write_b128 v100, v[52:55] offset:16\n"
      "v_mfma_f32_16x16x32_bf16 v[12:15], a[104:107], v[72:75], v[12:15]\n"
      "ds_read_b128 v[56:59], v101\n"
      "v_fma_f32 v24, v24, s55, v105\n"
      "v_mfma_f32_16x16x32_bf16 v[16:19], a[136:139], v[72:75], v[16:19]\n"
      "v_exp_f32 v24, v24\n"
      "v_mfma_f32_16x16x32_bf16 v[16:19], a[140:143], v[76:79], v[16:19]\n"
      "ds_read_b128 v[60:63], v101 offset:1152\n"
      "v_fma_f32 v25, v25, s55, v105\n"
      "v_mfma_f32_16x16x32_bf16 v[20:23], a[172:175], v[76:79], v[20:23]\n"
      "s_mul_i32 s59, s68, 2112\n"
      "v_exp_f32 v25, v25\n"
      "v_mfma_f32_16x16x32_bf16 v[20:23], a[168:171], v[72:75], v[20:23]\n"
      "s_waitcnt lgkmcnt(3)\n"
      "v_add_f32 v106, v24, v25\n"
      "v_cvt_pk_bf16_f32 v24, v24, v25\n"
      "v_mfma_f32_16x16x32_bf16 v[0:3], a[16:19], v[80:83], v[0:3]\n"
      "ds_read_b128 v[64:67], v98 offset:3072\n"
      "v_fma_f32 v26, v26, s55, v105\n"
      "v_mfma_f32_16x16x32_bf16 v[0:3], a[20:23], v[84:87], v[0:3]\n"
      "v_exp_f32 v26, v26\n"
      "v_mfma_f32_16x16x32_bf16 v[4:7], a[52:55], v[84:87], v[4:7]\n"
      "ds_read_b128 v[68:71], v98 offset:4096\n"
      "v_add_f32 v106, v106, v26\n"
      "v_mfma_f32_16x16x32_bf16 v[4:7], a[48:51], v[80:83], v[4:7]\n"
      "s_waitcnt lgkmcnt(2)\n"
      "v_fma_f32 v27, v27, s55, v105\n"
      "v_fma_f32 v28, v28, s55, v105\n"
      "v_mfma_f32_16x16x32_bf16 v[8:11], a[80:83], v[80:83], v[8:11]\n"
      "buffer_store_dwordx4 v[56:59], v103, s[48:51], s60 offen sc1\n"
      "v_cvt_f32_u32 v112, v112\n"
      "v_mfma_f32_16x16x32_bf16 v[8:11], a[84:87], v[84:87], v[8:11]\n"
      "s_add_u32 s60, s60, 8192\n"
      "v_exp_f32 v27, v27\n"
      "v_mfma_f32_16x16x32_bf16 v[12:15], a[116:119], v[84:87], v[12:15]\n"
      "v_add_f32 v106, v106, v27\n"
      "v_cvt_pk_bf16_f32 v25, v26, v27\n"
      "v_mfma_f32_16x16x32_bf16 v[12:15], a[112:115], v[80:83], v[12:15]\n"
      "v_exp_f32 v28, v28\n"
      "v_mfma_f32_16x16x32_bf16 v[16:19], a[144:147], v[80:83], v[16:19]\n"
      "v_add_f32 v106, v106, v28\n"
      "v_fma_f32 v29, v29, s55, v105\n"
      "v_mfma_f32_16x16x32_bf16 v[16:19], a[148:151], v[84:87], v[16:19]\n"
      "buffer_store_dwordx4 v[60:63], v103, s[48:51], s61 offen sc1\n"
      "v_fma_f32 v30, v30, s55, v105\n"
      "v_mfma_f32_16x16x32_bf16 v[20:23], a[180:183], v[84:87], v[20:23]\n"
      "s_add_u32 s61, s61, 8192\n"
      "v_rcp_f32 v107, v112\n"
      "v_mfma_f32_16x16x32_bf16 v[20:23], a[176:179], v[80:83], v[20:23]\n"
      "v_exp_f32 v29, v29\n"
      "v_mfma_f32_16x16x32_bf16 v[0:3], a[24:27], v[88:91], v[0:3]\n"
      "ds_read_b128 v[72:75], v98 offset:5120\n"
      "v_add_f32 v106, v106, v29\n"
      "v_mfma_f32_16x16x32_bf16 v[0:3], a[28:31], v[92:95], v[0:3]\n"
      "v_fma_f32 v31, v31, s55, v105\n"
      "v_cvt_pk_bf16_f32 v26, v28, v29\n"
      "v_mfma_f32_16x16x32_bf16 v[4:7], a[60:63], v[92:95], v[4:7]\n"
      "ds_read_b128 v[76:79], v98 offset:11520\n"
      "v_fma_f32 v32, v32, s55, v105\n"
      "v_mfma_f32_16x16x32_bf16 v[4:7], a[56:59], v[88:91], v[4:7]\n"
      "v_exp_f32 v30, v30\n"
      "v_mfma_f32_16x16x32_bf16 v[8:11], a[88:91], v[88:91], v[8:11]\n"
      "v_add_f32 v106, v106, v30\n"
      "v_fma_f32 v33, v33, s55, v105\n"
      "v_mfma_f32_16x16x32_bf16 v[8:11], a[92:95], v[92:95], v[8:11]\n"
      "v_exp_f32 v31, v31\n"
      "v_mfma_f32_16x16x32_bf16 v[12:15], a[124:127], v[92:95], v[12:15]\n"
      "v_add_f32 v106, v106, v31\n"
      "v_cvt_pk_bf16_f32 v27, v30, v31\n"
      "v_mfma_f32_16x16x32_bf16 v[12:15], a[120:123], v[88:91], v[12:15]\n"
      "ds_write_b128 v99, v[24:27]\n"
      "v_mfma_f32_16x16x32_bf16 v[16:19], a[152:155], v[88:91], v[16:19]\n"
      "s_waitcnt lgkmcnt(3)\n"
      "v_exp_f32 v32, v32\n"
      "v_mfma_f32_16x16x32_bf16 v[16:19], a[156:159], v[92:95], v[16:19]\n"
      "v_add_f32 v106, v106, v32\n"
      "v_fma_f32 v34, v34, s55, v105\n"
      "v_mfma_f32_16x16x32_bf16 v[20:23], a[188:191], v[92:95], v[20:23]\n"
      "v_exp_f32 v33, v33\n"
      "v_mfma_f32_16x16x32_bf16 v[20:23], a[184:187], v[88:91], v[20:23]\n"
      "v_add_f32 v106, v106, v33\n"
      "v_cvt_pk_bf16_f32 v32, v32, v33\n"
      "v_mfma_f32_16x16x32_bf16 v[48:51], a[192:195], v[64:67], 0\n"
      "ds_read_b128 v[80:83], v98 offset:12544\n"
      "v_max3_f32 v108, v0, v1, v2\n"
      "v_mfma_f32_16x16x32_bf16 v[48:51], a[208:211], v[68:71], v[48:51]\n"
      "buffer_load_dwordx4 v102, s[36:39], s57 offen nt lds\n"
      "v_mfma_f32_16x16x32_bf16 v[52:55], a[212:215], v[68:71], 0\n"
      "ds_read_b128 v[84:87], v98 offset:13568\n"
      "v_fma_f32 v35, v35, s55, v105\n"
      "v_mfma_f32_16x16x32_bf16 v[52:55], a[196:199], v[64:67], v[52:55]\n"
      "s_add_u32 m0, m0, 544\n"
      "v_exp_f32 v34, v34\n"
      "v_mfma_f32_16x16x32_bf16 v[56:59], a[200:203], v[64:67], 0\n"
      "s_waitcnt lgkmcnt(3)\n"
      "v_add_f32 v106, v106, v34\n"
      "v_max3_f32 v108, v108, v3, v4\n"
      "v_mfma_f32_16x16x32_bf16 v[56:59], a[216:219], v[68:71], v[56:59]\n"
      "buffer_load_dwordx4 v102, s[36:39], s57 offen offset:512 nt lds\n"
      "v_mfma_f32_16x16x32_bf16 v[60:63], a[220:223], v[68:71], 0\n"
      "s_add_u32 s57, s57, 8192\n"
      "v_exp_f32 v35, v35\n"
      "v_mfma_f32_16x16x32_bf16 v[60:63], a[204:207], v[64:67], v[60:63]\n"
      "v_add_f32 v106, v106, v35\n"
      "v_cvt_pk_bf16_f32 v33, v34, v35\n"
      "v_mfma_f32_16x16x32_bf16 v[48:51], a[224:227], v[72:75], v[48:51]\n"
      "ds_read_b128 v[88:91], v98 offset:19968\n"
      "v_fma_f32 v36, v36, s55, v105\n"
      "v_mfma_f32_16x16x32_bf16 v[48:51], a[240:243], v[76:79], v[48:51]\n"
      "v_max3_f32 v108, v108, v5, v6\n"
      "v_max3_f32 v108, v108, v7, v8\n"
      "v_mfma_f32_16x16x32_bf16 v[52:55], a[244:247], v[76:79], v[52:55]\n"
      "ds_read_b128 v[92:95], v98 offset:20992\n"
      "v_fma_f32 v37, v37, s55, v105\n"
      "v_mfma_f32_16x16x32_bf16 v[52:55], a[228:231], v[72:75], v[52:55]\n"
      "v_exp_f32 v36, v36\n"
      "v_mfma_f32_16x16x32_bf16 v[56:59], a[232:235], v[72:75], v[56:59]\n"
      "s_waitcnt lgkmcnt(2)\n"
      "v_add_f32 v106, v106, v36\n"
      "v_mfma_f32_16x16x32_bf16 v[56:59], a[248:251], v[76:79], v[56:59]\n"
      "v_exp_f32 v37, v37\n"
      "v_mfma_f32_16x16x32_bf16 v[60:63], a[252:255], v[76:79], v[60:63]\n"
      "v_add_f32 v106, v106, v37\n"
      "v_cvt_pk_bf16_f32 v34, v36, v37\n"
      "v_mfma_f32_16x16x32_bf16 v[60:63], a[236:239], v[72:75], v[60:63]\n"
      "v_fma_f32 v38, v38, s55, v105\n"
      "v_max3_f32 v108, v108, v9, v10\n"
      "v_mfma_f32_16x16x32_bf16 v[48:51], v[128:131], v[80:83], v[48:51]\n"
      "ds_read_b128 v[64:67], v98 offset:22016\n"
      "v_max3_f32 v108, v108, v11, v12\n"
      "v_mfma_f32_16x16x32_bf16 v[48:51], v[144:147], v[84:87], v[48:51]\n"
      "v_exp_f32 v38, v38\n"
      "v_mfma_f32_16x16x32_bf16 v[52:55], v[148:151], v[84:87], v[52:55]\n"
      "ds_read_b128 v[68:71], v98 offset:28416\n"
      "v_add_f32 v106, v106, v38\n"
      "v_mfma_f32_16x16x32_bf16 v[52:55], v[132:135], v[80:83], v[52:55]\n"
      "v_fma_f32 v39, v39, s55, v105\n"
      "v_max3_f32 v108, v108, v13, v14\n"
      "v_mfma_f32_16x16x32_bf16 v[56:59], v[136:139], v[80:83], v[56:59]\n"
      "s_waitcnt lgkmcnt(2)\n"
      "v_exp_f32 v39, v39\n"
      "v_mfma_f32_16x16x32_bf16 v[56:59], v[152:155], v[84:87], v[56:59]\n"
      "v_add_f32 v106, v106, v39\n"
      "v_cvt_pk_bf16_f32 v35, v38, v39\n"
      "v_mfma_f32_16x16x32_bf16 v[60:63], v[156:159], v[84:87], v[60:63]\n"
      "ds_write_b128 v99, v[32:35] offset:1024\n"
      "v_mfma_f32_16x16x32_bf16 v[60:63], v[140:143], v[80:83], v[60:63]\n"
      "v_add_u32 v96, s59, v97\n"
      "v_fma_f32 v40, v40, s55, v105\n"
      "v_mfma_f32_16x16x32_bf16 v[48:51], v[160:163], v[88:91], v[48:51]\n"
      "ds_read_b128 v[72:75], v98 offset:29440\n"
      "v_max3_f32 v108, v108, v15, v16\n"
      "v_mfma_f32_16x16x32_bf16 v[48:51], v[176:179], v[92:95], v[48:51]\n"
      "v_exp_f32 v40, v40\n"
      "v_mfma_f32_16x16x32_bf16 v[52:55], v[180:183], v[92:95], v[52:55]\n"
      "ds_read_b128 v[76:79], v98 offset:30464\n"
      "v_add_f32 v106, v106, v40\n"
      "v_mfma_f32_16x16x32_bf16 v[52:55], v[164:167], v[88:91], v[52:55]\n"
      "v_fma_f32 v41, v41, s55, v105\n"
      "v_max3_f32 v108, v108, v17, v18\n"
      "v_mfma_f32_16x16x32_bf16 v[56:59], v[168:171], v[88:91], v[56:59]\n"
      "s_waitcnt lgkmcnt(3)\n"
      "v_exp_f32 v41, v41\n"
      "v_mfma_f32_16x16x32_bf16 v[56:59], v[184:187], v[92:95], v[56:59]\n"
      "v_add_f32 v106, v106, v41\n"
      "v_cvt_pk_bf16_f32 v40, v40, v41\n"
      "v_mfma_f32_16x16x32_bf16 v[60:63], v[188:191], v[92:95], v[60:63]\n"
      "v_fma_f32 v42, v42, s55, v105\n"
      "v_max3_f32 v108, v108, v19, v20\n"
      "v_mfma_f32_16x16x32_bf16 v[60:63], v[172:175], v[88:91], v[60:63]\n"
      "v_exp_f32 v42, v42\n"
      "v_mfma_f32_16x16x32_bf16 v[48:51], v[192:195], v[64:67], v[48:51]\n"
      "ds_read_b128 v[80:83], v96\n"
      "v_add_f32 v106, v106, v42\n"
      "v_mfma_f32_16x16x32_bf16 v[48:51], v[208:211], v[68:71], v[48:51]\n"
      "v_fma_f32 v43, v43, s55, v105\n"
      "v_max3_f32 v108, v108, v21, v22\n"
      "v_mfma_f32_16x16x32_bf16 v[52:55], v[212:215], v[68:71], v[52:55]\n"
      "ds_read_b128 v[84:87], v96 offset:64\n"
      "v_fma_f32 v44, v44, s55, v105\n"
      "v_mfma_f32_16x16x32_bf16 v[52:55], v[196:199], v[64:67], v[52:55]\n"
      "v_exp_f32 v43, v43\n"
      "v_mfma_f32_16x16x32_bf16 v[56:59], v[200:203], v[64:67], v[56:59]\n"
      "s_waitcnt lgkmcnt(2)\n"
      "v_add_f32 v106, v106, v43\n"
      "v_cvt_pk_bf16_f32 v41, v42, v43\n"
      "v_mfma_f32_16x16x32_bf16 v[56:59], v[216:219], v[68:71], v[56:59]\n"
      "v_exp_f32 v44, v44\n"
      "v_mfma_f32_16x16x32_bf16 v[60:63], v[220:223], v[68:71], v[60:63]\n"
      "v_add_f32 v106, v106, v44\n"
      "v_max_f32 v108, v108, v23\n"
      "v_mfma_f32_16x16x32_bf16 v[60:63], v[204:207], v[64:67], v[60:63]\n"
      "ds_max_f32 v104, v108 offset:128\n"
      "v_mfma_f32_16x16x32_bf16 v[48:51], v[224:227], v[72:75], v[48:51]\n"
      "ds_read_b128 v[88:91], v96 offset:128\n"
      "v_fma_f32 v45, v45, s55, v105\n"
      "v_mfma_f32_16x16x32_bf16 v[48:51], v[240:243], v[76:79], v[48:51]\n"
      "v_exp_f32 v45, v45\n"
      "v_mfma_f32_16x16x32_bf16 v[52:55], v[244:247], v[76:79], v[52:55]\n"
      "ds_read_b128 v[92:95], v96 offset:192\n"
      "v_add_f32 v106, v106, v45\n"
      "v_mfma_f32_16x16x32_bf16 v[52:55], v[228:231], v[72:75], v[52:55]\n"
      "v_fma_f32 v46, v46, s55, v105\n"
      "v_cvt_pk_bf16_f32 v42, v44, v45\n"
      "v_mfma_f32_16x16x32_bf16 v[56:59], v[232:235], v[72:75], v[56:59]\n"
      "s_waitcnt lgkmcnt(3)\n"
      "v_exp_f32 v46, v46\n"
      "v_mfma_f32_16x16x32_bf16 v[56:59], v[248:251], v[76:79], v[56:59]\n"
      "v_add_f32 v106, v106, v46\n"
      "v_fma_f32 v47, v47, s55, v105\n"
      "v_mfma_f32_16x16x32_bf16 v[60:63], v[252:255], v[76:79], v[60:63]\n"
      "v_exp_f32 v47, v47\n"
      "v_mfma_f32_16x16x32_bf16 v[60:63], v[236:239], v[72:75], v[60:63]\n"
      "v_add_f32 v106, v106, v47\n"
      "v_cvt_pk_bf16_f32 v43, v46, v47\n"
      "v_mfma_f32_16x16x32_bf16 v[24:27], a[0:3], v[80:83], 0\n"
      "ds_read_b128 v[64:67], v96 offset:256\n"
      "v_cvt_u32_f32 v110, v106\n"
      "v_mfma_f32_16x16x32_bf16 v[24:27], a[4:7], v[84:87], v[24:27]\n"
      "ds_write_b128 v99, v[40:43] offset:2048\n"
      "ds_add_u32 v104, v110 offset:1600\n"
      "s_add_u32 s68, s68, 1\n"
      "v_mfma_f32_16x16x32_bf16 v[28:31], a[36:39], v[84:87], 0\n"
      "ds_read_b128 v[68:71], v96 offset:320\n"
      "v_mul_f32 v48, v107, v48\n"
      "v_mfma_f32_16x16x32_bf16 v[28:31], a[32:35], v[80:83], v[28:31]\n"
      "s_and_b32 s68, s68, 7\n"
      "v_mul_f32 v49, v107, v49\n"
      "v_cvt_pk_bf16_f32 v48, v48, v49\n"
      "v_mfma_f32_16x16x32_bf16 v[32:35], a[64:67], v[80:83], 0\n"
      "s_add_u32 s58, s68, 3\n"
      "v_mul_f32 v50, v107, v50\n"
      "v_mul_f32 v51, v107, v51\n"
      "v_mfma_f32_16x16x32_bf16 v[32:35], a[68:71], v[84:87], v[32:35]\n"
      "s_and_b32 s58, s58, 7\n"
      "v_cvt_pk_bf16_f32 v49, v50, v51\n"
      "v_mul_f32 v52, v107, v52\n"
      "v_mfma_f32_16x16x32_bf16 v[36:39], a[100:103], v[84:87], 0\n"
      "v_mul_f32 v53, v107, v53\n"
      "v_cvt_pk_bf16_f32 v50, v52, v53\n"
      "v_mfma_f32_16x16x32_bf16 v[36:39], a[96:99], v[80:83], v[36:39]\n"
      "s_mul_i32 s58, s58, 2112\n"
      "v_mul_f32 v54, v107, v54\n"
      "v_mul_f32 v55, v107, v55\n"
      "v_mfma_f32_16x16x32_bf16 v[40:43], a[128:131], v[80:83], 0\n"
      "s_sub_u32 s62, s62, 1\n"
      "v_cvt_pk_bf16_f32 v51, v54, v55\n"
      "v_mul_f32 v56, v107, v56\n"
      "v_mfma_f32_16x16x32_bf16 v[40:43], a[132:135], v[84:87], v[40:43]\n"
      "ds_write_b128 v100, v[48:51]\n"
      "v_mfma_f32_16x16x32_bf16 v[44:47], a[164:167], v[84:87], 0\n"
      "s_waitcnt vmcnt(8) lgkmcnt(2)\n"
      "s_barrier\n"
      "v_mul_f32 v57, v107, v57\n"
      "v_cvt_pk_bf16_f32 v52, v56, v57\n"
      "v_mfma_f32_16x16x32_bf16 v[44:47], a[160:163], v[80:83], v[44:47]\n"
      "ds_read_b32 v109, v104 offset:128\n"
      "v_mul_f32 v58, v107, v58\n"
      "v_mfma_f32_16x16x32_bf16 v[24:27], a[8:11], v[88:91], v[24:27]\n"
      "ds_read_b128 v[72:75], v96 offset:384\n"
      "v_mul_f32 v59, v107, v59\n"
      "v_mfma_f32_16x16x32_bf16 v[24:27], a[12:15], v[92:95], v[24:27]\n"
      "s_add_u32 m0, s53, s58\n"
      "v_cvt_pk_bf16_f32 v53, v58, v59\n"
      "v_mul_f32 v60, v107, v60\n"
      "v_mfma_f32_16x16x32_bf16 v[28:31], a[44:47], v[92:95], v[28:31]\n"
      "ds_read_b128 v[76:79], v96 offset:448\n"
      "v_mul_f32 v61, v107, v61\n"
      "v_mfma_f32_16x16x32_bf16 v[28:31], a[40:43], v[88:91], v[28:31]\n"
      "ds_read_b32 v112, v104 offset:1600\n"
      "v_cvt_pk_bf16_f32 v54, v60, v61\n"
      "v_mfma_f32_16x16x32_bf16 v[32:35], a[72:75], v[88:91], v[32:35]\n"
      "v_mul_f32 v62, v107, v62\n"
      "v_mul_f32 v63, v107, v63\n"
      "v_mfma_f32_16x16x32_bf16 v[32:35], a[76:79], v[92:95], v[32:35]\n"
      "s_waitcnt lgkmcnt(3)\n"
      "v_cvt_pk_bf16_f32 v55, v62, v63\n"
      "v_fma_f32 v105, v109, s56, v111\n"
      "v_mfma_f32_16x16x32_bf16 v[36:39], a[108:111], v[92:95], v[36:39]\n"
      "ds_write_b128 v100, v[52:55] offset:16\n"
      "v_mfma_f32_16x16x32_bf16 v[36:39], a[104:107], v[88:91], v[36:39]\n"
      "ds_read_b128 v[56:59], v101\n"
      "v_fma_f32 v0, v0, s55, v105\n"
      "v_mfma_f32_16x16x32_bf16 v[40:43], a[136:139], v[88:91], v[40:43]\n"
      "v_exp_f32 v0, v0\n"
      "v_mfma_f32_16x16x32_bf16 v[40:43], a[140:143], v[92:95], v[40:43]\n"
      "ds_read_b128 v[60:63], v101 offset:1152\n"
      "v_fma_f32 v1, v1, s55, v105\n"
      "v_mfma_f32_16x16x32_bf16 v[44:47], a[172:175], v[92:95], v[44:47]\n"
      "s_mul_i32 s59, s68, 2112\n"
      "v_exp_f32 v1, v1\n"
      "v_mfma_f32_16x16x32_bf16 v[44:47], a[168:171], v[88:91], v[44:47]\n"
      "s_waitcnt lgkmcnt(3)\n"
      "v_add_f32 v106, v0, v1\n"
      "v_cvt_pk_bf16_f32 v0, v0, v1\n"
      "v_mfma_f32_16x16x32_bf16 v[24:27], a[16:19], v[64:67], v[24:27]\n"
      "ds_read_b128 v[80:83], v98\n"
      "v_fma_f32 v2, v2, s55, v105\n"
      "v_mfma_f32_16x16x32_bf16 v[24:27], a[20:23], v[68:71], v[24:27]\n"
      "v_exp_f32 v2, v2\n"
      "v_mfma_f32_16x16x32_bf16 v[28:31], a[52:55], v[68:71], v[28:31]\n"
      "ds_read_b128 v[84:87], v98 offset:1024\n"
      "v_add_f32 v106, v106, v2\n"
      "v_mfma_f32_16x16x32_bf16 v[28:31], a[48:51], v[64:67], v[28:31]\n"
      "s_waitcnt lgkmcnt(2)\n"
      "v_fma_f32 v3, v3, s55, v105\n"
      "v_fma_f32 v4, v4, s55, v105\n"
      "v_mfma_f32_16x16x32_bf16 v[32:35], a[80:83], v[64:67], v[32:35]\n"
      "buffer_store_dwordx4 v[56:59], v103, s[48:51], s60 offen sc1\n"
      "v_cvt_f32_u32 v112, v112\n"
      "v_mfma_f32_16x16x32_bf16 v[32:35], a[84:87], v[68:71], v[32:35]\n"
      "s_add_u32 s60, s60, 8192\n"
      "v_exp_f32 v3, v3\n"
      "v_mfma_f32_16x16x32_bf16 v[36:39], a[116:119], v[68:71], v[36:39]\n"
      "v_add_f32 v106, v106, v3\n"
      "v_cvt_pk_bf16_f32 v1, v2, v3\n"
      "v_mfma_f32_16x16x32_bf16 v[36:39], a[112:115], v[64:67], v[36:39]\n"
      "v_exp_f32 v4, v4\n"
      "v_mfma_f32_16x16x32_bf16 v[40:43], a[144:147], v[64:67], v[40:43]\n"
      "v_add_f32 v106, v106, v4\n"
      "v_fma_f32 v5, v5, s55, v105\n"
      "v_mfma_f32_16x16x32_bf16 v[40:43], a[148:151], v[68:71], v[40:43]\n"
      "buffer_store_dwordx4 v[60:63], v103, s[48:51], s61 offen sc1\n"
      "v_fma_f32 v6, v6, s55, v105\n"
      "v_mfma_f32_16x16x32_bf16 v[44:47], a[180:183], v[68:71], v[44:47]\n"
      "s_add_u32 s61, s61, 8192\n"
      "v_rcp_f32 v107, v112\n"
      "v_mfma_f32_16x16x32_bf16 v[44:47], a[176:179], v[64:67], v[44:47]\n"
      "v_exp_f32 v5, v5\n"
      "v_mfma_f32_16x16x32_bf16 v[24:27], a[24:27], v[72:75], v[24:27]\n"
      "ds_read_b128 v[88:91], v98 offset:2048\n"
      "v_add_f32 v106, v106, v5\n"
      "v_mfma_f32_16x16x32_bf16 v[24:27], a[28:31], v[76:79], v[24:27]\n"
      "v_fma_f32 v7, v7, s55, v105\n"
      "v_cvt_pk_bf16_f32 v2, v4, v5\n"
      "v_mfma_f32_16x16x32_bf16 v[28:31], a[60:63], v[76:79], v[28:31]\n"
      "ds_read_b128 v[92:95], v98 offset:8448\n"
      "v_fma_f32 v8, v8, s55, v105\n"
      "v_mfma_f32_16x16x32_bf16 v[28:31], a[56:59], v[72:75], v[28:31]\n"
      "v_exp_f32 v6, v6\n"
      "v_mfma_f32_16x16x32_bf16 v[32:35], a[88:91], v[72:75], v[32:35]\n"
      "v_add_f32 v106, v106, v6\n"
      "v_fma_f32 v9, v9, s55, v105\n"
      "v_mfma_f32_16x16x32_bf16 v[32:35], a[92:95], v[76:79], v[32:35]\n"
      "v_exp_f32 v7, v7\n"
      "v_mfma_f32_16x16x32_bf16 v[36:39], a[124:127], v[76:79], v[36:39]\n"
      "v_add_f32 v106, v106, v7\n"
      "v_cvt_pk_bf16_f32 v3, v6, v7\n"
      "v_mfma_f32_16x16x32_bf16 v[36:39], a[120:123], v[72:75], v[36:39]\n"
      "ds_write_b128 v99, v[0:3] offset:3072\n"
      "v_mfma_f32_16x16x32_bf16 v[40:43], a[152:155], v[72:75], v[40:43]\n"
      "s_waitcnt lgkmcnt(3)\n"
      "v_exp_f32 v8, v8\n"
      "v_mfma_f32_16x16x32_bf16 v[40:43], a[156:159], v[76:79], v[40:43]\n"
      "v_add_f32 v106, v106, v8\n"
      "v_fma_f32 v10, v10, s55, v105\n"
      "v_mfma_f32_16x16x32_bf16 v[44:47], a[188:191], v[76:79], v[44:47]\n"
      "v_exp_f32 v9, v9\n"
      "v_mfma_f32_16x16x32_bf16 v[44:47], a[184:187], v[72:75], v[44:47]\n"
      "v_add_f32 v106, v106, v9\n"
      "v_cvt_pk_bf16_f32 v8, v8, v9\n"
      "v_mfma_f32_16x16x32_bf16 v[48:51], a[192:195], v[80:83], 0\n"
      "ds_read_b128 v[64:67], v98 offset:9472\n"
      "v_max3_f32 v108, v24, v25, v26\n"
      "v_mfma_f32_16x16x32_bf16 v[48:51], a[208:211], v[84:87], v[48:51]\n"
      "buffer_load_dwordx4 v102, s[36:39], s57 offen nt lds\n"
      "v_mfma_f32_16x16x32_bf16 v[52:55], a[212:215], v[84:87], 0\n"
      "ds_read_b128 v[68:71], v98 offset:10496\n"
      "v_fma_f32 v11, v11, s55, v105\n"
      "v_mfma_f32_16x16x32_bf16 v[52:55], a[196:199], v[80:83], v[52:55]\n"
      "s_add_u32 m0, m0, 544\n"
      "v_exp_f32 v10, v10\n"
      "v_mfma_f32_16x16x32_bf16 v[56:59], a[200:203], v[80:83], 0\n"
      "s_waitcnt lgkmcnt(3)\n"
      "v_add_f32 v106, v106, v10\n"
      "v_max3_f32 v108, v108, v27, v28\n"
      "v_mfma_f32_16x16x32_bf16 v[56:59], a[216:219], v[84:87], v[56:59]\n"
      "buffer_load_dwordx4 v102, s[36:39], s57 offen offset:512 nt lds\n"
      "v_mfma_f32_16x16x32_bf16 v[60:63], a[220:223], v[84:87], 0\n"
      "s_add_u32 s57, s57, 8192\n"
      "v_exp_f32 v11, v11\n"
      "v_mfma_f32_16x16x32_bf16 v[60:63], a[204:207], v[80:83], v[60:63]\n"
      "v_add_f32 v106, v106, v11\n"
      "v_cvt_pk_bf16_f32 v9, v10, v11\n"
      "v_mfma_f32_16x16x32_bf16 v[48:51], a[224:227], v[88:91], v[48:51]\n"
      "ds_read_b128 v[72:75], v98 offset:16896\n"
      "v_fma_f32 v12, v12, s55, v105\n"
      "v_mfma_f32_16x16x32_bf16 v[48:51], a[240:243], v[92:95], v[48:51]\n"
      "v_max3_f32 v108, v108, v29, v30\n"
      "v_max3_f32 v108, v108, v31, v32\n"
      "v_mfma_f32_16x16x32_bf16 v[52:55], a[244:247], v[92:95], v[52:55]\n"
      "ds_read_b128 v[76:79], v98 offset:17920\n"
      "v_fma_f32 v13, v13, s55, v105\n"
      "v_mfma_f32_16x16x32_bf16 v[52:55], a[228:231], v[88:91], v[52:55]\n"
      "v_exp_f32 v12, v12\n"
      "v_mfma_f32_16x16x32_bf16 v[56:59], a[232:235], v[88:91], v[56:59]\n"
      "s_waitcnt lgkmcnt(2)\n"
      "v_add_f32 v106, v106, v12\n"
      "v_add_u32 v104, 128, v104\n"
      "v_mfma_f32_16x16x32_bf16 v[56:59], a[248:251], v[92:95], v[56:59]\n"
      "v_exp_f32 v13, v13\n"
      "v_mfma_f32_16x16x32_bf16 v[60:63], a[252:255], v[92:95], v[60:63]\n"
      "v_add_f32 v106, v106, v13\n"
      "v_cvt_pk_bf16_f32 v10, v12, v13\n"
      "v_mfma_f32_16x16x32_bf16 v[60:63], a[236:239], v[88:91], v[60:63]\n"
      "v_fma_f32 v14, v14, s55, v105\n"
      "v_max3_f32 v108, v108, v33, v34\n"
      "v_mfma_f32_16x16x32_bf16 v[48:51], v[128:131], v[64:67], v[48:51]\n"
      "ds_read_b128 v[80:83], v98 offset:18944\n"
      "v_max3_f32 v108, v108, v35, v36\n"
      "v_mfma_f32_16x16x32_bf16 v[48:51], v[144:147], v[68:71], v[48:51]\n"
      "v_exp_f32 v14, v14\n"
      "v_mfma_f32_16x16x32_bf16 v[52:55], v[148:151], v[68:71], v[52:55]\n"
      "ds_read_b128 v[84:87], v98 offset:25344\n"
      "v_add_f32 v106, v106, v14\n"
      "v_mfma_f32_16x16x32_bf16 v[52:55], v[132:135], v[64:67], v[52:55]\n"
      "v_fma_f32 v15, v15, s55, v105\n"
      "v_max3_f32 v108, v108, v37, v38\n"
      "v_mfma_f32_16x16x32_bf16 v[56:59], v[136:139], v[64:67], v[56:59]\n"
      "s_waitcnt lgkmcnt(2)\n"
      "v_exp_f32 v15, v15\n"
      "v_mfma_f32_16x16x32_bf16 v[56:59], v[152:155], v[68:71], v[56:59]\n"
      "v_add_f32 v106, v106, v15\n"
      "v_cvt_pk_bf16_f32 v11, v14, v15\n"
      "v_mfma_f32_16x16x32_bf16 v[60:63], v[156:159], v[68:71], v[60:63]\n"
      "ds_write_b128 v99, v[8:11] offset:4096\n"
      "v_mfma_f32_16x16x32_bf16 v[60:63], v[140:143], v[64:67], v[60:63]\n"
      "v_add_u32 v96, s59, v97\n"
      "v_fma_f32 v16, v16, s55, v105\n"
      "v_mfma_f32_16x16x32_bf16 v[48:51], v[160:163], v[72:75], v[48:51]\n"
      "ds_read_b128 v[88:91], v98 offset:26368\n"
      "v_max3_f32 v108, v108, v39, v40\n"
      "v_mfma_f32_16x16x32_bf16 v[48:51], v[176:179], v[76:79], v[48:51]\n"
      "v_exp_f32 v16, v16\n"
      "v_mfma_f32_16x16x32_bf16 v[52:55], v[180:183], v[76:79], v[52:55]\n"
      "ds_read_b128 v[92:95], v98 offset:27392\n"
      "v_add_f32 v106, v106, v16\n"
      "v_mfma_f32_16x16x32_bf16 v[52:55], v[164:167], v[72:75], v[52:55]\n"
      "v_fma_f32 v17, v17, s55, v105\n"
      "v_max3_f32 v108, v108, v41, v42\n"
      "v_mfma_f32_16x16x32_bf16 v[56:59], v[168:171], v[72:75], v[56:59]\n"
      "s_waitcnt lgkmcnt(3)\n"
      "v_exp_f32 v17, v17\n"
      "v_mfma_f32_16x16x32_bf16 v[56:59], v[184:187], v[76:79], v[56:59]\n"
      "v_add_f32 v106, v106, v17\n"
      "v_cvt_pk_bf16_f32 v16, v16, v17\n"
      "v_mfma_f32_16x16x32_bf16 v[60:63], v[188:191], v[76:79], v[60:63]\n"
      "v_fma_f32 v18, v18, s55, v105\n"
      "v_max3_f32 v108, v108, v43, v44\n"
      "v_mfma_f32_16x16x32_bf16 v[60:63], v[172:175], v[72:75], v[60:63]\n"
      "v_exp_f32 v18, v18\n"
      "v_mfma_f32_16x16x32_bf16 v[48:51], v[192:195], v[80:83], v[48:51]\n"
      "ds_read_b128 v[64:67], v96\n"
      "v_add_f32 v106, v106, v18\n"
      "v_mfma_f32_16x16x32_bf16 v[48:51], v[208:211], v[84:87], v[48:51]\n"
      "v_fma_f32 v19, v19, s55, v105\n"
      "v_max3_f32 v108, v108, v45, v46\n"
      "v_mfma_f32_16x16x32_bf16 v[52:55], v[212:215], v[84:87], v[52:55]\n"
      "ds_read_b128 v[68:71], v96 offset:64\n"
      "v_fma_f32 v20, v20, s55, v105\n"
      "v_mfma_f32_16x16x32_bf16 v[52:55], v[196:199], v[80:83], v[52:55]\n"
      "v_exp_f32 v19, v19\n"
      "v_mfma_f32_16x16x32_bf16 v[56:59], v[200:203], v[80:83], v[56:59]\n"
      "s_waitcnt lgkmcnt(2)\n"
      "v_add_f32 v106, v106, v19\n"
      "v_cvt_pk_bf16_f32 v17, v18, v19\n"
      "v_mfma_f32_16x16x32_bf16 v[56:59], v[216:219], v[84:87], v[56:59]\n"
      "v_exp_f32 v20, v20\n"
      "v_mfma_f32_16x16x32_bf16 v[60:63], v[220:223], v[84:87], v[60:63]\n"
      "v_add_f32 v106, v106, v20\n"
      "v_max_f32 v108, v108, v47\n"
      "v_mfma_f32_16x16x32_bf16 v[60:63], v[204:207], v[80:83], v[60:63]\n"
      "ds_max_f32 v104, v108 offset:64\n"
      "v_mfma_f32_16x16x32_bf16 v[48:51], v[224:227], v[88:91], v[48:51]\n"
      "ds_read_b128 v[72:75], v96 offset:128\n"
      "v_fma_f32 v21, v21, s55, v105\n"
      "v_mfma_f32_16x16x32_bf16 v[48:51], v[240:243], v[92:95], v[48:51]\n"
      "v_exp_f32 v21, v21\n"
      "v_mfma_f32_16x16x32_bf16 v[52:55], v[244:247], v[92:95], v[52:55]\n"
      "ds_read_b128 v[76:79], v96 offset:192\n"
      "v_add_f32 v106, v106, v21\n"
      "v_mfma_f32_16x16x32_bf16 v[52:55], v[228:231], v[88:91], v[52:55]\n"
      "v_fma_f32 v22, v22, s55, v105\n"
      "v_cvt_pk_bf16_f32 v18, v20, v21\n"
      "v_mfma_f32_16x16x32_bf16 v[56:59], v[232:235], v[88:91], v[56:59]\n"
      "s_waitcnt lgkmcnt(3)\n"
      "v_exp_f32 v22, v22\n"
      "v_mfma_f32_16x16x32_bf16 v[56:59], v[248:251], v[92:95], v[56:59]\n"
      "s_cmp_lg_u32 s62, 0\n"
      "v_add_f32 v106, v106, v22\n"
      "v_fma_f32 v23, v23, s55, v105\n"
      "v_mfma_f32_16x16x32_bf16 v[60:63], v[252:255], v[92:95], v[60:63]\n"
      "v_exp_f32 v23, v23\n"
      "v_mfma_f32_16x16x32_bf16 v[60:63], v[236:239], v[88:91], v[60:63]\n"
      "v_add_f32 v106, v106, v23\n"
      "v_cvt_pk_bf16_f32 v19, v22, v23\n"
      "s_cbranch_scc1 8b\n"
      "v_cvt_u32_f32 v110, v106\n"
      "ds_write_b128 v99, v[16:19] offset:5120\n"
      "ds_add_u32 v104, v110 offset:1536\n"
      "s_waitcnt lgkmcnt(2)\n"
      "s_waitcnt lgkmcnt(0)\n"
      "s_barrier\n"
      "ds_read_b128 v[64:67], v98 offset:3072\n"
      "ds_read_b128 v[68:71], v98 offset:4096\n"
      "ds_read_b128 v[72:75], v98 offset:5120\n"
      "ds_read_b128 v[76:79], v98 offset:11520\n"
      "s_waitcnt lgkmcnt(3)\n"
      "v_mfma_f32_16x16x32_bf16 v[0:3], a[192:195], v[64:67], 0\n"
      "ds_read_b32 v109, v104 offset:64\n"
      "s_waitcnt lgkmcnt(3)\n"
      "v_mfma_f32_16x16x32_bf16 v[0:3], a[208:211], v[68:71], v[0:3]\n"
      "ds_read_b128 v[80:83], v98 offset:12544\n"
      "v_mfma_f32_16x16x32_bf16 v[4:7], a[212:215], v[68:71], 0\n"
      "ds_read_b128 v[84:87], v98 offset:13568\n"
      "v_mfma_f32_16x16x32_bf16 v[4:7], a[196:199], v[64:67], v[4:7]\n"
      "ds_read_b32 v108, v104 offset:1536\n"
      "v_mfma_f32_16x16x32_bf16 v[8:11], a[200:203], v[64:67], 0\n"
      "v_mfma_f32_16x16x32_bf16 v[8:11], a[216:219], v[68:71], v[8:11]\n"
      "v_mfma_f32_16x16x32_bf16 v[12:15], a[220:223], v[68:71], 0\n"
      "s_waitcnt lgkmcnt(3)\n"
      "v_fma_f32 v105, v109, s56, v111\n"
      "v_mfma_f32_16x16x32_bf16 v[12:15], a[204:207], v[64:67], v[12:15]\n"
      "v_fma_f32 v24, v24, s55, v105\n"
      "v_fma_f32 v25, v25, s55, v105\n"
      "v_mfma_f32_16x16x32_bf16 v[0:3], a[224:227], v[72:75], v[0:3]\n"
      "ds_read_b128 v[88:91], v98 offset:19968\n"
      "v_exp_f32 v24, v24\n"
      "v_mfma_f32_16x16x32_bf16 v[0:3], a[240:243], v[76:79], v[0:3]\n"
      "v_exp_f32 v25, v25\n"
      "v_mfma_f32_16x16x32_bf16 v[4:7], a[244:247], v[76:79], v[4:7]\n"
      "ds_read_b128 v[92:95], v98 offset:20992\n"
      "v_add_f32 v106, v24, v25\n"
      "v_mfma_f32_16x16x32_bf16 v[4:7], a[228:231], v[72:75], v[4:7]\n"
      "s_waitcnt lgkmcnt(2)\n"
      "v_cvt_pk_bf16_f32 v24, v24, v25\n"
      "v_fma_f32 v26, v26, s55, v105\n"
      "v_mfma_f32_16x16x32_bf16 v[8:11], a[232:235], v[72:75], v[8:11]\n"
      "v_exp_f32 v26, v26\n"
      "v_fma_f32 v27, v27, s55, v105\n"
      "v_mfma_f32_16x16x32_bf16 v[8:11], a[248:251], v[76:79], v[8:11]\n"
      "v_add_f32 v106, v106, v26\n"
      "v_exp_f32 v27, v27\n"
      "v_mfma_f32_16x16x32_bf16 v[12:15], a[252:255], v[76:79], v[12:15]\n"
      "v_add_f32 v106, v106, v27\n"
      "v_cvt_pk_bf16_f32 v25, v26, v27\n"
      "v_fma_f32 v28, v28, s55, v105\n"
      "v_mfma_f32_16x16x32_bf16 v[12:15], a[236:239], v[72:75], v[12:15]\n"
      "v_exp_f32 v28, v28\n"
      "v_fma_f32 v29, v29, s55, v105\n"
      "v_mfma_f32_16x16x32_bf16 v[0:3], v[128:131], v[80:83], v[0:3]\n"
      "ds_read_b128 v[64:67], v98 offset:22016\n"
      "v_add_f32 v106, v106, v28\n"
      "v_exp_f32 v29, v29\n"
      "v_mfma_f32_16x16x32_bf16 v[0:3], v[144:147], v[84:87], v[0:3]\n"
      "v_add_f32 v106, v106, v29\n"
      "v_cvt_pk_bf16_f32 v26, v28, v29\n"
      "v_fma_f32 v30, v30, s55, v105\n"
      "v_mfma_f32_16x16x32_bf16 v[4:7], v[148:151], v[84:87], v[4:7]\n"
      "ds_read_b128 v[68:71], v98 offset:28416\n"
      "v_exp_f32 v30, v30\n"
      "v_fma_f32 v31, v31, s55, v105\n"
      "v_mfma_f32_16x16x32_bf16 v[4:7], v[132:135], v[80:83], v[4:7]\n"
      "v_add_f32 v106, v106, v30\n"
      "v_exp_f32 v31, v31\n"
      "v_mfma_f32_16x16x32_bf16 v[8:11], v[136:139], v[80:83], v[8:11]\n"
      "v_add_f32 v106, v106, v31\n"
      "v_cvt_pk_bf16_f32 v27, v30, v31\n"
      "v_fma_f32 v32, v32, s55, v105\n"
      "v_mfma_f32_16x16x32_bf16 v[8:11], v[152:155], v[84:87], v[8:11]\n"
      "ds_write_b128 v99, v[24:27]\n"
      "v_exp_f32 v32, v32\n"
      "v_fma_f32 v33, v33, s55, v105\n"
      "v_mfma_f32_16x16x32_bf16 v[12:15], v[156:159], v[84:87], v[12:15]\n"
      "v_add_f32 v106, v106, v32\n"
      "v_exp_f32 v33, v33\n"
      "v_mfma_f32_16x16x32_bf16 v[12:15], v[140:143], v[80:83], v[12:15]\n"
      "v_add_f32 v106, v106, v33\n"
      "v_cvt_pk_bf16_f32 v32, v32, v33\n"
      "v_fma_f32 v34, v34, s55, v105\n"
      "s_waitcnt lgkmcnt(4)\n"
      "v_mfma_f32_16x16x32_bf16 v[0:3], v[160:163], v[88:91], v[0:3]\n"
      "ds_read_b128 v[72:75], v98 offset:29440\n"
      "v_exp_f32 v34, v34\n"
      "v_fma_f32 v35, v35, s55, v105\n"
      "s_waitcnt lgkmcnt(4)\n"
      "v_mfma_f32_16x16x32_bf16 v[0:3], v[176:179], v[92:95], v[0:3]\n"
      "v_add_f32 v106, v106, v34\n"
      "v_exp_f32 v35, v35\n"
      "v_mfma_f32_16x16x32_bf16 v[4:7], v[180:183], v[92:95], v[4:7]\n"
      "ds_read_b128 v[76:79], v98 offset:30464\n"
      "v_add_f32 v106, v106, v35\n"
      "v_cvt_pk_bf16_f32 v33, v34, v35\n"
      "v_fma_f32 v36, v36, s55, v105\n"
      "v_mfma_f32_16x16x32_bf16 v[4:7], v[164:167], v[88:91], v[4:7]\n"
      "v_exp_f32 v36, v36\n"
      "v_mfma_f32_16x16x32_bf16 v[8:11], v[168:171], v[88:91], v[8:11]\n"
      "v_add_f32 v106, v106, v36\n"
      "v_fma_f32 v37, v37, s55, v105\n"
      "v_mfma_f32_16x16x32_bf16 v[8:11], v[184:187], v[92:95], v[8:11]\n"
      "v_exp_f32 v37, v37\n"
      "v_fma_f32 v38, v38, s55, v105\n"
      "v_mfma_f32_16x16x32_bf16 v[12:15], v[188:191], v[92:95], v[12:15]\n"
      "v_add_f32 v106, v106, v37\n"
      "v_cvt_pk_bf16_f32 v34, v36, v37\n"
      "v_exp_f32 v38, v38\n"
      "v_mfma_f32_16x16x32_bf16 v[12:15], v[172:175], v[88:91], v[12:15]\n"
      "v_add_f32 v106, v106, v38\n"
      "v_fma_f32 v39, v39, s55, v105\n"
      "s_waitcnt lgkmcnt(4)\n"
      "v_mfma_f32_16x16x32_bf16 v[0:3], v[192:195], v[64:67], v[0:3]\n"
      "v_exp_f32 v39, v39\n"
      "v_fma_f32 v40, v40, s55, v105\n"
      "s_waitcnt lgkmcnt(3)\n"
      "v_mfma_f32_16x16x32_bf16 v[0:3], v[208:211], v[68:71], v[0:3]\n"
      "v_add_f32 v106, v106, v39\n"
      "v_cvt_pk_bf16_f32 v35, v38, v39\n"
      "v_exp_f32 v40, v40\n"
      "v_mfma_f32_16x16x32_bf16 v[4:7], v[212:215], v[68:71], v[4:7]\n"
      "ds_write_b128 v99, v[32:35] offset:1024\n"
      "v_add_f32 v106, v106, v40\n"
      "v_fma_f32 v41, v41, s55, v105\n"
      "v_mfma_f32_16x16x32_bf16 v[4:7], v[196:199], v[64:67], v[4:7]\n"
      "v_exp_f32 v41, v41\n"
      "v_fma_f32 v42, v42, s55, v105\n"
      "v_mfma_f32_16x16x32_bf16 v[8:11], v[200:203], v[64:67], v[8:11]\n"
      "v_add_f32 v106, v106, v41\n"
      "v_cvt_pk_bf16_f32 v40, v40, v41\n"
      "v_exp_f32 v42, v42\n"
      "v_mfma_f32_16x16x32_bf16 v[8:11], v[216:219], v[68:71], v[8:11]\n"
      "v_add_f32 v106, v106, v42\n"
      "v_fma_f32 v43, v43, s55, v105\n"
      "v_mfma_f32_16x16x32_bf16 v[12:15], v[220:223], v[68:71], v[12:15]\n"
      "v_exp_f32 v43, v43\n"
      "v_fma_f32 v44, v44, s55, v105\n"
      "v_mfma_f32_16x16x32_bf16 v[12:15], v[204:207], v[64:67], v[12:15]\n"
      "v_add_f32 v106, v106, v43\n"
      "v_cvt_pk_bf16_f32 v41, v42, v43\n"
      "v_exp_f32 v44, v44\n"
      "s_waitcnt lgkmcnt(2)\n"
      "v_mfma_f32_16x16x32_bf16 v[0:3], v[224:227], v[72:75], v[0:3]\n"
      "v_add_f32 v106, v106, v44\n"
      "v_fma_f32 v45, v45, s55, v105\n"
      "s_waitcnt lgkmcnt(1)\n"
      "v_mfma_f32_16x16x32_bf16 v[0:3], v[240:243], v[76:79], v[0:3]\n"
      "v_exp_f32 v45, v45\n"
      "v_fma_f32 v46, v46, s55, v105\n"
      "v_mfma_f32_16x16x32_bf16 v[4:7], v[244:247], v[76:79], v[4:7]\n"
      "v_add_f32 v106, v106, v45\n"
      "v_cvt_pk_bf16_f32 v42, v44, v45\n"
      "v_exp_f32 v46, v46\n"
      "v_mfma_f32_16x16x32_bf16 v[4:7], v[228:231], v[72:75], v[4:7]\n"
      "v_add_f32 v106, v106, v46\n"
      "v_fma_f32 v47, v47, s55, v105\n"
      "v_mfma_f32_16x16x32_bf16 v[8:11], v[232:235], v[72:75], v[8:11]\n"
      "v_exp_f32 v47, v47\n"
      "v_mfma_f32_16x16x32_bf16 v[8:11], v[248:251], v[76:79], v[8:11]\n"
      "v_add_f32 v106, v106, v47\n"
      "v_cvt_pk_bf16_f32 v43, v46, v47\n"
      "v_cvt_f32_u32 v108, v108\n"
      "v_mfma_f32_16x16x32_bf16 v[12:15], v[252:255], v[76:79], v[12:15]\n"
      "ds_write_b128 v99, v[40:43] offset:2048\n"
      "v_cvt_u32_f32 v110, v106\n"
      "v_rcp_f32 v108, v108\n"
      "v_mfma_f32_16x16x32_bf16 v[12:15], v[236:239], v[72:75], v[12:15]\n"
      "ds_add_u32 v104, v110 offset:1600\n"
      "s_waitcnt lgkmcnt(0)\n"
      "s_barrier\n"
      "ds_read_b128 v[80:83], v98\n"
      "ds_read_b128 v[84:87], v98 offset:1024\n"
      "ds_read_b128 v[88:91], v98 offset:2048\n"
      "ds_read_b128 v[92:95], v98 offset:8448\n"
      "s_waitcnt lgkmcnt(3)\n"
      "v_mfma_f32_16x16x32_bf16 v[24:27], a[192:195], v[80:83], 0\n"
      "ds_read_b128 v[64:67], v98 offset:9472\n"
      "v_mul_f32 v48, v107, v48\n"
      "s_waitcnt lgkmcnt(3)\n"
      "v_mfma_f32_16x16x32_bf16 v[24:27], a[208:211], v[84:87], v[24:27]\n"
      "ds_read_b32 v109, v104 offset:1600\n"
      "v_mul_f32 v49, v107, v49\n"
      "v_mfma_f32_16x16x32_bf16 v[28:31], a[212:215], v[84:87], 0\n"
      "ds_read_b128 v[68:71], v98 offset:10496\n"
      "v_cvt_pk_bf16_f32 v48, v48, v49\n"
      "v_mfma_f32_16x16x32_bf16 v[28:31], a[196:199], v[80:83], v[28:31]\n"
      "v_mul_f32 v50, v107, v50\n"
      "v_mul_f32 v51, v107, v51\n"
      "v_mfma_f32_16x16x32_bf16 v[32:35], a[200:203], v[80:83], 0\n"
      "v_cvt_pk_bf16_f32 v49, v50, v51\n"
      "v_mul_f32 v52, v107, v52\n"
      "v_mfma_f32_16x16x32_bf16 v[32:35], a[216:219], v[84:87], v[32:35]\n"
      "v_mul_f32 v53, v107, v53\n"
      "v_cvt_pk_bf16_f32 v50, v52, v53\n"
      "v_mfma_f32_16x16x32_bf16 v[36:39], a[220:223], v[84:87], 0\n"
      "v_mul_f32 v54, v107, v54\n"
      "v_mul_f32 v55, v107, v55\n"
      "v_mfma_f32_16x16x32_bf16 v[36:39], a[204:207], v[80:83], v[36:39]\n"
      "v_cvt_pk_bf16_f32 v51, v54, v55\n"
      "v_mul_f32 v56, v107, v56\n"
      "s_waitcnt lgkmcnt(4)\n"
      "v_mfma_f32_16x16x32_bf16 v[24:27], a[224:227], v[88:91], v[24:27]\n"
      "ds_read_b128 v[72:75], v98 offset:16896\n"
      "v_mul_f32 v57, v107, v57\n"
      "s_waitcnt lgkmcnt(4)\n"
      "v_mfma_f32_16x16x32_bf16 v[24:27], a[240:243], v[92:95], v[24:27]\n"
      "ds_write_b128 v100, v[48:51]\n"
      "v_mfma_f32_16x16x32_bf16 v[28:31], a[244:247], v[92:95], v[28:31]\n"
      "ds_read_b128 v[76:79], v98 offset:17920\n"
      "v_cvt_pk_bf16_f32 v52, v56, v57\n"
      "v_mfma_f32_16x16x32_bf16 v[28:31], a[228:231], v[88:91], v[28:31]\n"
      "s_waitcnt lgkmcnt(4)\n"
      "v_mul_f32 v58, v107, v58\n"
      "v_mul_f32 v59, v107, v59\n"
      "v_mfma_f32_16x16x32_bf16 v[32:35], a[232:235], v[88:91], v[32:35]\n"
      "v_cvt_pk_bf16_f32 v53, v58, v59\n"
      "v_mul_f32 v60, v107, v60\n"
      "v_mfma_f32_16x16x32_bf16 v[32:35], a[248:251], v[92:95], v[32:35]\n"
      "v_mul_f32 v61, v107, v61\n"
      "v_cvt_pk_bf16_f32 v54, v60, v61\n"
      "v_mfma_f32_16x16x32_bf16 v[36:39], a[252:255], v[92:95], v[36:39]\n"
      "v_mul_f32 v62, v107, v62\n"
      "v_mul_f32 v63, v107, v63\n"
      "v_mfma_f32_16x16x32_bf16 v[36:39], a[236:239], v[88:91], v[36:39]\n"
      "v_cvt_pk_bf16_f32 v55, v62, v63\n"
      "v_mul_f32 v0, v108, v0\n"
      "v_mfma_f32_16x16x32_bf16 v[24:27], v[128:131], v[64:67], v[24:27]\n"
      "ds_read_b128 v[80:83], v98 offset:18944\n"
      "v_mul_f32 v1, v108, v1\n"
      "s_waitcnt lgkmcnt(4)\n"
      "v_mfma_f32_16x16x32_bf16 v[24:27], v[144:147], v[68:71], v[24:27]\n"
      "ds_write_b128 v100, v[52:55] offset:16\n"
      "v_mfma_f32_16x16x32_bf16 v[28:31], v[148:151], v[68:71], v[28:31]\n"
      "ds_read_b128 v[84:87], v98 offset:25344\n"
      "v_cvt_pk_bf16_f32 v0, v0, v1\n"
      "v_mfma_f32_16x16x32_bf16 v[28:31], v[132:135], v[64:67], v[28:31]\n"
      "ds_read_b128 v[56:59], v101\n"
      "v_mul_f32 v2, v108, v2\n"
      "v_mfma_f32_16x16x32_bf16 v[32:35], v[136:139], v[64:67], v[32:35]\n"
      "ds_read_b128 v[60:63], v101 offset:1152\n"
      "v_mul_f32 v3, v108, v3\n"
      "v_mfma_f32_16x16x32_bf16 v[32:35], v[152:155], v[68:71], v[32:35]\n"
      "v_cvt_pk_bf16_f32 v1, v2, v3\n"
      "v_mul_f32 v4, v108, v4\n"
      "v_mfma_f32_16x16x32_bf16 v[36:39], v[156:159], v[68:71], v[36:39]\n"
      "v_mul_f32 v5, v108, v5\n"
      "v_cvt_pk_bf16_f32 v2, v4, v5\n"
      "v_mfma_f32_16x16x32_bf16 v[36:39], v[140:143], v[64:67], v[36:39]\n"
      "v_mul_f32 v6, v108, v6\n"
      "v_mul_f32 v7, v108, v7\n"
      "s_waitcnt lgkmcnt(7)\n"
      "v_mfma_f32_16x16x32_bf16 v[24:27], v[160:163], v[72:75], v[24:27]\n"
      "ds_read_b128 v[88:91], v98 offset:26368\n"
      "v_cvt_pk_bf16_f32 v3, v6, v7\n"
      "s_waitcnt lgkmcnt(6)\n"
      "v_mfma_f32_16x16x32_bf16 v[24:27], v[176:179], v[76:79], v[24:27]\n"
      "ds_write_b128 v100, v[0:3]\n"
      "v_mfma_f32_16x16x32_bf16 v[28:31], v[180:183], v[76:79], v[28:31]\n"
      "ds_read_b128 v[92:95], v98 offset:27392\n"
      "v_mul_f32 v8, v108, v8\n"
      "v_mfma_f32_16x16x32_bf16 v[28:31], v[164:167], v[72:75], v[28:31]\n"
      "s_waitcnt lgkmcnt(3)\n"
      "v_mul_f32 v9, v108, v9\n"
      "v_cvt_pk_bf16_f32 v4, v8, v9\n"
      "v_mfma_f32_16x16x32_bf16 v[32:35], v[168:171], v[72:75], v[32:35]\n"
      "buffer_store_dwordx4 v[56:59], v103, s[48:51], s60 offen sc1\n"
      "v_mul_f32 v10, v108, v10\n"
      "v_mfma_f32_16x16x32_bf16 v[32:35], v[184:187], v[76:79], v[32:35]\n"
      "buffer_store_dwordx4 v[60:63], v103, s[48:51], s61 offen sc1\n"
      "v_mul_f32 v11, v108, v11\n"
      "v_mfma_f32_16x16x32_bf16 v[36:39], v[188:191], v[76:79], v[36:39]\n"
      "s_add_u32 s60, s60, 8192\n"
      "v_cvt_pk_bf16_f32 v5, v10, v11\n"
      "v_mul_f32 v12, v108, v12\n"
      "v_mfma_f32_16x16x32_bf16 v[36:39], v[172:175], v[72:75], v[36:39]\n"
      "s_add_u32 s61, s61, 8192\n"
      "v_mul_f32 v13, v108, v13\n"
      "v_cvt_pk_bf16_f32 v6, v12, v13\n"
      "v_mfma_f32_16x16x32_bf16 v[24:27], v[192:195], v[80:83], v[24:27]\n"
      "v_mul_f32 v14, v108, v14\n"
      "v_mul_f32 v15, v108, v15\n"
      "v_mfma_f32_16x16x32_bf16 v[24:27], v[208:211], v[84:87], v[24:27]\n"
      "v_cvt_pk_bf16_f32 v7, v14, v15\n"
      "v_cvt_f32_u32 v109, v109\n"
      "v_mfma_f32_16x16x32_bf16 v[28:31], v[212:215], v[84:87], v[28:31]\n"
      "ds_write_b128 v100, v[4:7] offset:16\n"
      "v_mfma_f32_16x16x32_bf16 v[28:31], v[196:199], v[80:83], v[28:31]\n"
      "ds_read_b128 v[8:11], v101\n"
      "v_mfma_f32_16x16x32_bf16 v[32:35], v[200:203], v[80:83], v[32:35]\n"
      "ds_read_b128 v[12:15], v101 offset:1152\n"
      "v_mfma_f32_16x16x32_bf16 v[32:35], v[216:219], v[84:87], v[32:35]\n"
      "v_rcp_f32 v109, v109\n"
      "v_mfma_f32_16x16x32_bf16 v[36:39], v[220:223], v[84:87], v[36:39]\n"
      "v_mfma_f32_16x16x32_bf16 v[36:39], v[204:207], v[80:83], v[36:39]\n"
      "s_waitcnt lgkmcnt(5)\n"
      "v_mfma_f32_16x16x32_bf16 v[24:27], v[224:227], v[88:91], v[24:27]\n"
      "s_waitcnt lgkmcnt(3)\n"
      "v_mfma_f32_16x16x32_bf16 v[24:27], v[240:243], v[92:95], v[24:27]\n"
      "v_mfma_f32_16x16x32_bf16 v[28:31], v[244:247], v[92:95], v[28:31]\n"
      "s_waitcnt lgkmcnt(0)\n"
      "v_mfma_f32_16x16x32_bf16 v[28:31], v[228:231], v[88:91], v[28:31]\n"
      "buffer_store_dwordx4 v[8:11], v103, s[48:51], s60 offen sc1\n"
      "v_mfma_f32_16x16x32_bf16 v[32:35], v[232:235], v[88:91], v[32:35]\n"
      "buffer_store_dwordx4 v[12:15], v103, s[48:51], s61 offen sc1\n"
      "v_mfma_f32_16x16x32_bf16 v[32:35], v[248:251], v[92:95], v[32:35]\n"
      "s_add_u32 s60, s60, 8192\n"
      "v_mfma_f32_16x16x32_bf16 v[36:39], v[252:255], v[92:95], v[36:39]\n"
      "s_add_u32 s61, s61, 8192\n"
      "v_mfma_f32_16x16x32_bf16 v[36:39], v[236:239], v[88:91], v[36:39]\n"
      "v_mul_f32 v24, v109, v24\n"
      "v_mul_f32 v25, v109, v25\n"
      "v_cvt_pk_bf16_f32 v24, v24, v25\n"
      "v_mul_f32 v26, v109, v26\n"
      "v_mul_f32 v27, v109, v27\n"
      "v_cvt_pk_bf16_f32 v25, v26, v27\n"
      "v_mul_f32 v28, v109, v28\n"
      "v_mul_f32 v29, v109, v29\n"
      "v_cvt_pk_bf16_f32 v26, v28, v29\n"
      "v_mul_f32 v30, v109, v30\n"
      "v_mul_f32 v31, v109, v31\n"
      "v_cvt_pk_bf16_f32 v27, v30, v31\n"
      "v_mul_f32 v32, v109, v32\n"
      "v_mul_f32 v33, v109, v33\n"
      "v_cvt_pk_bf16_f32 v28, v32, v33\n"
      "v_mul_f32 v34, v109, v34\n"
      "v_mul_f32 v35, v109, v35\n"
      "v_cvt_pk_bf16_f32 v29, v34, v35\n"
      "v_mul_f32 v36, v109, v36\n"
      "v_mul_f32 v37, v109, v37\n"
      "v_cvt_pk_bf16_f32 v30, v36, v37\n"
      "v_mul_f32 v38, v109, v38\n"
      "v_mul_f32 v39, v109, v39\n"
      "v_cvt_pk_bf16_f32 v31, v38, v39\n"
      "ds_write_b128 v100, v[24:27]\n"
      "ds_write_b128 v100, v[28:31] offset:16\n"
      "ds_read_b128 v[32:35], v101\n"
      "ds_read_b128 v[36:39], v101 offset:1152\n"
      "s_waitcnt lgkmcnt(0)\n"
      "buffer_store_dwordx4 v[32:35], v103, s[48:51], s60 offen sc1\n"
      "buffer_store_dwordx4 v[36:39], v103, s[48:51], s61 offen sc1\n"
      "s_add_u32 s60, s60, 8192\n"
      "s_add_u32 s61, s61, 8192\n"
      "9:\n"
      :
      : "v"(threadIdx.x), "s"((unsigned)qa), "s"((unsigned)(qa >> 32)), "s"((unsigned)ka), "s"((unsigned)(ka >> 32)),
        "s"((unsigned)va), "s"((unsigned)(va >> 32)), "s"((unsigned)oa), "s"((unsigned)(oa >> 32)), "s"(blockIdx.x)
      : "v1", "v2", "v3", "v4", "v5", "v6", "v7", "v8", "v9", "v10", "v11", "v12", "v13", "v14", "v15", "v16", "v17", "v18", "v19", "v20", "v21", "v22", "v23", "v24", "v25", "v26", "v27", "v28", "v29", "v30", "v31", "v32", "v33", "v34", "v35", "v36", "v37", "v38", "v39", "v40", "v41", "v42", "v43", "v44", "v45", "v46", "v47", "v48", "v49", "v50", "v51", "v52", "v53", "v54", "v55", "v56", "v57", "v58", "v59", "v60", "v61", "v62", "v63", "v64", "v65", "v66", "v67", "v68", "v69", "v70", "v71", "v72", "v73", "v74", "v75", "v76", "v77", "v78", "v79", "v80", "v81", "v82", "v83", "v84", "v85", "v86", "v87", "v88", "v89", "v90", "v91", "v92", "v93", "v94", "v95", "v96", "v97", "v98", "v99", "v100", "v101", "v102", "v103", "v104", "v105", "v106", "v107", "v108", "v109", "v110", "v111", "v112", "v113", "v114", "v115", "v116", "v117", "v118", "v119", "v120", "v121", "v122", "v123", "v124", "v125", "v126", "v127", "v128", "v129", "v130", "v131", "v132", "v133", "v134", "v135", "v136", "v137", "v138", "v139", "v140", "v141", "v142", "v143", "v144", "v145", "v146", "v147", "v148", "v149", "v150", "v151", "v152", "v153", "v154", "v155", "v156", "v157", "v158", "v159", "v160", "v161", "v162", "v163", "v164", "v165", "v166", "v167", "v168", "v169", "v170", "v171", "v172", "v173", "v174", "v175", "v176", "v177", "v178", "v179", "v180", "v181", "v182", "v183", "v184", "v185", "v186", "v187", "v188", "v189", "v190", "v191", "v192", "v193", "v194", "v195", "v196", "v197", "v198", "v199", "v200", "v201", "v202", "v203", "v204", "v205", "v206", "v207", "v208", "v209", "v210", "v211", "v212", "v213", "v214", "v215", "v216", "v217", "v218", "v219", "v220", "v221", "v222", "v223", "v224", "v225", "v226", "v227", "v228", "v229", "v230", "v231", "v232", "v233", "v234", "v235", "v236", "v237", "v238", "v239", "v240", "v241", "v242", "v243", "v244", "v245", "v246", "v247", "v248", "v249", "v250", "v251", "v252", "v253", "v254", "v255", "a0", "a1", "a2", "a3", "a4", "a5", "a6", "a7", "a8", "a9", "a10", "a11", "a12", "a13", "a14", "a15", "a16", "a17", "a18", "a19", "a20", "a21", "a22", "a23", "a24", "a25", "a26", "a27", "a28", "a29", "a30", "a31", "a32", "a33", "a34", "a35", "a36", "a37", "a38", "a39", "a40", "a41", "a42", "a43", "a44", "a45", "a46", "a47", "a48", "a49", "a50", "a51", "a52", "a53", "a54", "a55", "a56", "a57", "a58", "a59", "a60", "a61", "a62", "a63", "a64", "a65", "a66", "a67", "a68", "a69", "a70", "a71", "a72", "a73", "a74", "a75", "a76", "a77", "a78", "a79", "a80", "a81", "a82", "a83", "a84", "a85", "a86", "a87", "a88", "a89", "a90", "a91", "a92", "a93", "a94", "a95", "a96", "a97", "a98", "a99", "a100", "a101", "a102", "a103", "a104", "a105", "a106", "a107", "a108", "a109", "a110", "a111", "a112", "a113", "a114", "a115", "a116", "a117", "a118", "a119", "a120", "a121", "a122", "a123", "a124", "a125", "a126", "a127", "a128", "a129", "a130", "a131", "a132", "a133", "a134", "a135", "a136", "a137", "a138", "a139", "a140", "a141", "a142", "a143", "a144", "a145", "a146", "a147", "a148", "a149", "a150", "a151", "a152", "a153", "a154", "a155", "a156", "a157", "a158", "a159", "a160", "a161", "a162", "a163", "a164", "a165", "a166", "a167", "a168", "a169", "a170", "a171", "a172", "a173", "a174", "a175", "a176", "a177", "a178", "a179", "a180", "a181", "a182", "a183", "a184", "a185", "a186", "a187", "a188", "a189", "a190", "a191", "a192", "a193", "a194", "a195", "a196", "a197", "a198", "a199", "a200", "a201", "a202", "a203", "a204", "a205", "a206", "a207", "a208", "a209", "a210", "a211", "a212", "a213", "a214", "a215", "a216", "a217", "a218", "a219", "a220", "a221", "a222", "a223", "a224", "a225", "a226", "a227", "a228", "a229", "a230", "a231", "a232", "a233", "a234", "a235", "a236", "a237", "a238", "a239", "a240", "a241", "a242", "a243", "a244", "a245", "a246", "a247", "a248", "a249", "a250", "a251", "a252", "a253", "a254", "a255", "s36", "s37", "s38", "s39", "s40", "s41", "s42", "s43", "s44", "s45", "s46", "s47", "s48", "s49", "s50", "s51", "s52", "s53", "s54", "s55", "s56", "s57", "s58", "s59", "s60", "s61", "s62", "s63", "s64", "s65", "s66", "s67", "s68", "s69", "s70", "s71", "s72", "s73", "s74", "s75", "s76", "s77", "s78", "s79", "s80", "s81", "s82", "s83", "s84", "s85", "s86", "s87", "s88", "s89", "s90", "s91", "s92", "s93", "s94", "s95", "s96", "s97", "s98", "s99", "vcc", "scc", "memory");
}

extern "C" void kernel_launch(void* const* d_in, const int* in_sizes, int n_in, void* d_out, int out_size, void* d_ws,
                              size_t ws_size, hipStream_t stream) {
  gqa_kvs_kernel<<<256, 256, 0, stream>>>(d_in[0], d_in[1], d_in[2], d_out);
}
```
